```python
import torch

def _softmax(x, axis=-1):
    return torch.softmax(x, dim=axis)
def _gelu(x):
    return torch.nn.functional.gelu(x, approximate="tanh")
def _silu(x):
    return torch.nn.functional.silu(x)
def _sigmoid(x):
    return torch.sigmoid(x)
def _layernorm(x, axis=-1, eps=1e-5):
    m = x.mean(dim=axis, keepdim=True)
    v = x.var(dim=axis, keepdim=True, unbiased=False)
    return (x - m) / torch.sqrt(v + eps)
def _scatter_add(tgt, idx, src, axis=0):
    return tgt.clone().index_add_(axis, idx, src)
def _segment_sum(src, seg, n):
    out = torch.zeros((n,) + src.shape[1:], dtype=src.dtype)
    return out.index_add_(0, seg, src)
def _pad(x, pw):
    flat = tuple(v for pair in reversed(pw) for v in pair)
    return torch.nn.functional.pad(x, flat)

kernel_name = 'standalone__adm_resblock__diffusion'

def setup_inputs():

    _w = (lambda a: (lambda t: t.to(torch.bfloat16) if t.is_floating_point() else t)(torch.as_tensor(a)))
    B, C, HW, E = 2, 320, 64, 1152
    sc = (9.0 * C) ** -0.5
    return {
        "x": _w((lambda *s: torch.randn(*s, dtype=torch.bfloat16) * 0.05)(B, C, HW, HW)),
        "t_emb": _w((lambda *s: torch.randn(*s, dtype=torch.bfloat16) * 0.2)(B, E)),
        "g1": _w(1.0 + 0.1 * (lambda *s: torch.randn(*s, dtype=torch.bfloat16) * 0.2)(C)),
        "be1": _w(0.1 * (lambda *s: torch.randn(*s, dtype=torch.bfloat16) * 0.2)(C)),
        "w1": _w((lambda *s: torch.randn(*s, dtype=torch.bfloat16) * 0.2)(C, C, 3, 3) * sc),
        "w_emb": _w((lambda *s: torch.randn(*s, dtype=torch.bfloat16) * 0.2)(E, C) * (E ** -0.5)),
        "g2": _w(1.0 + 0.1 * (lambda *s: torch.randn(*s, dtype=torch.bfloat16) * 0.2)(C)),
        "be2": _w(0.1 * (lambda *s: torch.randn(*s, dtype=torch.bfloat16) * 0.2)(C)),
        "w2": _w((lambda *s: torch.randn(*s, dtype=torch.bfloat16) * 0.2)(C, C, 3, 3) * sc),
    }

def reference(x, t_emb, g1, be1, w1, w_emb, g2, be2, w2):
    B, C, H, W = x.shape
    G, eps = 4, 1e-5
    def gn(z, gamma, beta):
        zr = z.reshape(B, G, (C // G) * H * W)
        mu = zr.mean(-1).reshape(B, G, 1)
        var = ((zr - mu) ** 2).mean(-1).reshape(B, G, 1)
        zn = ((zr - mu) / torch.sqrt(var + eps)).reshape(B, C, H, W)
        return zn * gamma.reshape(1, C, 1, 1) + beta.reshape(1, C, 1, 1)
    def conv3(z, w):
        zp = _pad(z, ((0, 0), (0, 0), (1, 1), (1, 1)))
        taps = [torch.einsum("bchw,oc->bohw", zp[:, :, i:i+H, j:j+W], w[:, :, i, j]) for i in range(3) for j in range(3)]
        out = taps[0]
        for t in taps[1:]:
            out = out + t
        return out
    h = conv3(_silu(gn(x, g1, be1)), w1)
    emb = _silu(t_emb) @ w_emb
    h = h + emb.reshape(B, C, 1, 1)
    h = conv3(_silu(gn(h, g2, be2)), w2)
    return x + h
```

```cpp
#include <hip/hip_runtime.h>
#include <hip/hip_bf16.h>
#include <stdint.h>
#include <utility>

typedef __attribute__((ext_vector_type(8))) __bf16 bf16x8;
typedef __attribute__((ext_vector_type(2))) __bf16 bf16x2;
typedef __attribute__((ext_vector_type(4))) float f32x4;
typedef __attribute__((ext_vector_type(2))) float f32x2;
typedef __attribute__((ext_vector_type(4))) unsigned int u32x4;
typedef __attribute__((ext_vector_type(2))) unsigned int u32x2;
#define LDS_AS __attribute__((address_space(3)))
#define DEVI __device__ __forceinline__

constexpr int NCH = 320, NPX = 4096, NEMB = 1152;
constexpr int PLANE_B = NPX * 32;
constexpr int ACT_B = 2 * 20 * PLANE_B;
constexpr int NSTEP = 23;
constexpr int WQ_B = NSTEP * 5 * 1024;
constexpr int WT_B = 16 * WQ_B;
constexpr size_t OFF_ACT1 = 0;
constexpr size_t OFF_ACT2 = OFF_ACT1 + ACT_B;
constexpr size_t OFF_WT1 = OFF_ACT2 + ACT_B;
constexpr size_t OFF_WT2 = OFF_WT1 + WT_B;
constexpr size_t OFF_EMB = OFF_WT2 + WT_B;
constexpr size_t OFF_ST1 = OFF_EMB + 4 * 2 * 320 * 4;
constexpr size_t OFF_ST2 = OFF_ST1 + 8 * 160 * 8;

template <typename F, int... I>
DEVI void sfor_impl(F&& f, std::integer_sequence<int, I...>) { (f(std::integral_constant<int, I>{}), ...); }
template <int N, typename F>
DEVI void sfor(F&& f) { sfor_impl(f, std::make_integer_sequence<int, N>{}); }

DEVI float bf_lo(unsigned v) { return __uint_as_float(v << 16); }
DEVI float bf_hi(unsigned v) { return __uint_as_float(v & 0xffff0000u); }
DEVI unsigned pk_bf16(float a, float b) {
  bf16x2 r = __builtin_convertvector((f32x2){a, b}, bf16x2);
  return __builtin_bit_cast(unsigned, r);
}
DEVI float silu_f(float y) {
  float e = __builtin_amdgcn_exp2f(y * -1.44269504088896341f);
  return y * __builtin_amdgcn_rcpf(1.0f + e);
}
template <int CTRL>
DEVI float dpp_add(float v) {
  return v + __builtin_bit_cast(float, __builtin_amdgcn_update_dpp(0, __builtin_bit_cast(int, v), CTRL, 0xf, 0xf, true));
}
DEVI float wave_sum(float v) {
  v = dpp_add<0xB1>(v);
  v = dpp_add<0x4E>(v);
  v = dpp_add<0x141>(v);
  v = dpp_add<0x140>(v);
  int iv = __builtin_bit_cast(int, v);
  float a = __builtin_bit_cast(float, __builtin_amdgcn_readlane(iv, 0));
  float b = __builtin_bit_cast(float, __builtin_amdgcn_readlane(iv, 16));
  float c = __builtin_bit_cast(float, __builtin_amdgcn_readlane(iv, 32));
  float d = __builtin_bit_cast(float, __builtin_amdgcn_readlane(iv, 48));
  return (a + b) + (c + d);
}

template <int NENT>
DEVI void combine_stats(const float* __restrict__ st, int lane, float& mu, float& rstd) {
  constexpr float CNT = 327680.0f / NENT;
  f32x2 e[3];
  float s = 0.f;
#pragma unroll
  for (int k = 0; k < 3; ++k) {
    int idx = lane + 64 * k;
    if (64 * k < NENT) {
      bool ok = idx < NENT;
      f32x2 v = *(const f32x2*)(st + 2 * (ok ? idx : 0));
      e[k] = v;
      s += ok ? v.x : 0.f;
    }
  }
  mu = wave_sum(s) * (1.0f / NENT);
  float m2 = 0.f;
#pragma unroll
  for (int k = 0; k < 3; ++k) {
    int idx = lane + 64 * k;
    if (64 * k < NENT) {
      float d = e[k].x - mu;
      m2 += (idx < NENT) ? (e[k].y + CNT * d * d) : 0.f;
    }
  }
  float var = wave_sum(m2) * (1.0f / 327680.0f);
  rstd = 1.0f / sqrtf(var + 1e-5f);
}

__global__ __launch_bounds__(320) void prep_k(const uint16_t* __restrict__ x, const uint16_t* __restrict__ temb,
                                              const uint16_t* __restrict__ w1, const uint16_t* __restrict__ wemb,
                                              const uint16_t* __restrict__ w2, char* __restrict__ ws) {
  __shared__ float red[5 * 64];
  const int bid = blockIdx.x, tid = threadIdx.x, lane = tid & 63;
  const int wv = __builtin_amdgcn_readfirstlane(tid >> 6);
  if (bid < 256) {
    const int bg = bid >> 5, j = bid & 31;
    const unsigned* src = (const unsigned*)(x + (size_t)(bg * 80 + wv * 16) * NPX + j * 128) + lane;
    unsigned v[16];
#pragma unroll
    for (int c = 0; c < 16; ++c) v[c] = src[c * (NPX / 2)];
    float s = 0.f;
#pragma unroll
    for (int c = 0; c < 16; ++c) s += bf_lo(v[c]) + bf_hi(v[c]);
    const float mean = wave_sum(s) * (1.0f / 2048.0f);
    float m2 = 0.f;
#pragma unroll
    for (int c = 0; c < 16; ++c) {
      float a = bf_lo(v[c]) - mean, b = bf_hi(v[c]) - mean;
      m2 += a * a + b * b;
    }
    m2 = wave_sum(m2);
    if (lane == 0) *(f32x2*)(ws + OFF_ST1 + (bg * 160 + j * 5 + wv) * 8) = (f32x2){mean, m2};
  } else if (bid < 256 + 80) {
    const int job = (bid - 256) * 5 + wv;
    const int cv = job / 200, r = job % 200;
    const int ot = r / 10, oc = (r % 10) * 4 + (lane >> 4);
    const int o = ot * 16 + (lane & 15);
    const uint16_t* w = cv ? w2 : w1;
    const u32x4* src = (const u32x4*)(w + (size_t)o * 2880 + oc * 72);
    unsigned in[36];
#pragma unroll
    for (int k = 0; k < 9; ++k) {
      u32x4 t = src[k];
      in[4 * k] = t.x; in[4 * k + 1] = t.y; in[4 * k + 2] = t.z; in[4 * k + 3] = t.w;
    }
    const int kq = oc / 10, rr = oc % 10, slab = rr >> 1, u = rr & 1;
    const int ns = ot / 5, n = ot % 5;
    char* dst = ws + (cv ? OFF_WT2 : OFF_WT1) + (size_t)(ns * 4 + kq) * WQ_B + n * 1024 + (lane & 15) * 16 + u * 256;
    const bool single = slab == 0;
    const int pbase = (5 + 9 * ((slab - 1) >> 1)) * 5120 + ((slab - 1) & 1) * 512;
    sfor<9>([&](auto T) {
      constexpr int t = T, dy = t / 3, dx = t % 3;
      constexpr int sstep = dy < 2 ? dx : (dx == 0 ? 3 : 4);
      constexpr int shi = dy < 2 ? dy : (dx == 2 ? 1 : 0);
      u32x4 ov;
      sfor<4>([&](auto Q) {
        constexpr int q = Q, e0 = 9 * (2 * q) + t, e1 = 9 * (2 * q + 1) + t;
        constexpr unsigned sel = (unsigned)((e0 & 1) * 2) | (unsigned)((e0 & 1) * 2 + 1) << 8 |
                                 (unsigned)(4 + (e1 & 1) * 2) << 16 | (unsigned)(4 + (e1 & 1) * 2 + 1) << 24;
        ov[q] = __builtin_amdgcn_perm(in[e1 >> 1], in[e0 >> 1], sel);
      });
      const int off = single ? sstep * 5120 + shi * 512 : pbase + t * 5120;
      *(u32x4*)(dst + off) = ov;
    });
    if (single) *(u32x4*)(dst + 3 * 5120 + 512) = (u32x4){0u, 0u, 0u, 0u};
  } else {
    const int r = bid - 336, kq = r / 10, cg = r % 10;
    float acc[2][8];
#pragma unroll
    for (int i = 0; i < 8; ++i) acc[0][i] = acc[1][i] = 0.f;
    if (tid < 288) {
      const int row0 = kq * 288 + (tid >> 2), col = cg * 32 + (tid & 3) * 8;
      u32x4 wr[4];
      float t0[4], t1[4];
#pragma unroll
      for (int k = 0; k < 4; ++k) {
        const int row = row0 + 72 * k;
        wr[k] = *(const u32x4*)(wemb + (size_t)row * NCH + col);
        t0[k] = bf_lo(temb[row]);
        t1[k] = bf_lo(temb[NEMB + row]);
      }
#pragma unroll
      for (int k = 0; k < 4; ++k) {
        const float s0 = silu_f(t0[k]), s1 = silu_f(t1[k]);
#pragma unroll
        for (int q = 0; q < 4; ++q) {
          const float a = bf_lo(wr[k][q]), b = bf_hi(wr[k][q]);
          acc[0][2 * q] += s0 * a; acc[0][2 * q + 1] += s0 * b;
          acc[1][2 * q] += s1 * a; acc[1][2 * q + 1] += s1 * b;
        }
      }
    }
#pragma unroll
    for (int b = 0; b < 2; ++b)
#pragma unroll
      for (int i = 0; i < 8; ++i) {
        float v = acc[b][i];
        v += __shfl_xor(v, 4); v += __shfl_xor(v, 8); v += __shfl_xor(v, 16); v += __shfl_xor(v, 32);
        if (lane < 4) red[wv * 64 + b * 32 + lane * 8 + i] = v;
      }
    __syncthreads();
    if (tid < 64) {
      float v = red[tid] + red[64 + tid] + red[128 + tid] + red[192 + tid] + red[256 + tid];
      ((float*)(ws + OFF_EMB))[(kq * 2 + (tid >> 5)) * NCH + cg * 32 + (tid & 31)] = v;
    }
  }
}

__global__ __launch_bounds__(320) void act1_k(const uint16_t* __restrict__ x, const uint16_t* __restrict__ gam,
                                              const uint16_t* __restrict__ bet, char* __restrict__ ws) {
  __shared__ __attribute__((aligned(16))) char tr[5 * 4096];
  const int bid = blockIdx.x, tid = threadIdx.x, lane = tid & 63;
  const int wv = __builtin_amdgcn_readfirstlane(tid >> 6);
  const int bg = bid >> 5, j = bid & 31;
  const unsigned* src = (const unsigned*)(x + (size_t)(bg * 80 + wv * 16) * NPX + j * 128) + lane;
  unsigned v[16];
#pragma unroll
  for (int c = 0; c < 16; ++c) v[c] = src[c * (NPX / 2)];
  float mu, rstd;
  combine_stats<160>((const float*)(ws + OFF_ST1) + bg * 320, lane, mu, rstd);
  const int ch0 = (bg & 3) * 80 + wv * 16;
  unsigned o[2][8];
#pragma unroll
  for (int c = 0; c < 16; c += 2) {
    const unsigned gg = *(const unsigned*)(gam + ch0 + c), bb = *(const unsigned*)(bet + ch0 + c);
    const float a0 = rstd * bf_lo(gg), a1 = rstd * bf_hi(gg);
    const float b0 = bf_lo(bb) - mu * a0, b1 = bf_hi(bb) - mu * a1;
    o[0][c >> 1] = pk_bf16(silu_f(bf_lo(v[c]) * a0 + b0), silu_f(bf_lo(v[c + 1]) * a1 + b1));
    o[1][c >> 1] = pk_bf16(silu_f(bf_hi(v[c]) * a0 + b0), silu_f(bf_hi(v[c + 1]) * a1 + b1));
  }
  char* t = tr + wv * 4096;
#pragma unroll
  for (int k = 0; k < 4; ++k) {
    u32x4 q = {o[k >> 1][(k & 1) * 4], o[k >> 1][(k & 1) * 4 + 1], o[k >> 1][(k & 1) * 4 + 2], o[k >> 1][(k & 1) * 4 + 3]};
    *(u32x4*)(t + lane * 64 + ((k ^ ((lane >> 2) & 3)) << 4)) = q;
  }
  __builtin_amdgcn_wave_barrier();
  char* dst = ws + OFF_ACT1 + (size_t)(bg * 5 + wv) * PLANE_B + j * 4096 + lane * 16;
#pragma unroll
  for (int m = 0; m < 4; ++m) {
    u32x4 q = *(const u32x4*)(t + m * 1024 + ((lane & ~3) << 4) + (((lane & 3) ^ ((lane >> 4) & 3)) << 4));
    *(u32x4*)(dst + m * 1024) = q;
  }
}

__global__ __launch_bounds__(320) void act2_k(const uint16_t* __restrict__ gam, const uint16_t* __restrict__ bet,
                                              char* __restrict__ ws) {
  const int bid = blockIdx.x, tid = threadIdx.x, lane = tid & 63;
  const int wv = __builtin_amdgcn_readfirstlane(tid >> 6);
  const int bg = bid >> 5, j = bid & 31;
  char* p = ws + OFF_ACT2 + (size_t)(bg * 5 + wv) * PLANE_B + j * 4096 + lane * 16;
  u32x4 v[4];
#pragma unroll
  for (int m = 0; m < 4; ++m) v[m] = *(const u32x4*)(p + m * 1024);
  float mu, rstd;
  combine_stats<128>((const float*)(ws + OFF_ST2) + bg * 256, lane, mu, rstd);
  const int ch0 = (bg & 3) * 80 + wv * 16 + (lane & 1) * 8;
  const u32x4 gg = *(const u32x4*)(gam + ch0), bb = *(const u32x4*)(bet + ch0);
  float a[8], b[8];
#pragma unroll
  for (int q = 0; q < 4; ++q) {
    a[2 * q] = rstd * bf_lo(gg[q]); a[2 * q + 1] = rstd * bf_hi(gg[q]);
    b[2 * q] = bf_lo(bb[q]) - mu * a[2 * q]; b[2 * q + 1] = bf_hi(bb[q]) - mu * a[2 * q + 1];
  }
#pragma unroll
  for (int m = 0; m < 4; ++m) {
    u32x4 o;
#pragma unroll
    for (int q = 0; q < 4; ++q)
      o[q] = pk_bf16(silu_f(bf_lo(v[m][q]) * a[2 * q] + b[2 * q]), silu_f(bf_hi(v[m][q]) * a[2 * q + 1] + b[2 * q + 1]));
    *(u32x4*)(p + m * 1024) = o;
  }
}

constexpr int TF = 4;
constexpr int PD = 4;
constexpr int LEAD = 7;
constexpr int PRO = 26;
constexpr int NG = NSTEP * 8;
constexpr int GF = (NSTEP - TF) * 8;
constexpr int NOP = 150;
constexpr int SLAB_B = 6144, WREG_B = 40960;

struct Sched {
  int ty[NOP], oa[NOP], ob[NOP];
  int opBeg[NG + 2];
  int gs[NG], gr[NG];
  int wW[NG], wD[NG + PD], tieW[NG];
  int wAux;
};
constexpr int step_slab_last(int s) { return s < 5 ? 0 : (s < 14 ? 2 : 4); }
constexpr Sched make_sched(int mode) {
  Sched S{};
  int fu[NSTEP] = {};
  for (int g = 0; g < NG; ++g) {
    if (g < GF) { S.gs[g] = g / 8; S.gr[g] = g % 8; }
    else { int k = g - GF; S.gr[g] = k / TF; S.gs[g] = NSTEP - TF + k % TF; }
  }
  for (int s = 0; s < NSTEP; ++s) fu[s] = s < NSTEP - TF ? 8 * s : GF + (s - (NSTEP - TF));
  int dl[NOP] = {};
  int n = 0;
  for (int p = 0; p < 6; ++p) { S.ty[n] = 0; S.oa[n] = 0; S.ob[n] = p; dl[n] = -10; ++n; }
  for (int k = 0; k < 5; ++k) { S.ty[n] = 2; S.oa[n] = k; S.ob[n] = 0; dl[n] = mode == 1 ? 0 : GF + 8; ++n; }
  for (int s = 0; s < NSTEP; ++s)
    for (int k = 0; k < 5; ++k) { S.ty[n] = 1; S.oa[n] = s; S.ob[n] = k; dl[n] = fu[s]; ++n; }
  for (int sl = 1; sl < 5; ++sl)
    for (int p = 0; p < 6; ++p) { S.ty[n] = 0; S.oa[n] = sl; S.ob[n] = p; dl[n] = (sl < 3 ? fu[5] : fu[14]) - PD - 12; ++n; }
  for (int i = 1; i < NOP; ++i) {
    int t = S.ty[i], a = S.oa[i], b = S.ob[i], d = dl[i], j = i - 1;
    while (j >= 0 && dl[j] > d) { S.ty[j + 1] = S.ty[j]; S.oa[j + 1] = S.oa[j]; S.ob[j + 1] = S.ob[j]; dl[j + 1] = dl[j]; --j; }
    S.ty[j + 1] = t; S.oa[j + 1] = a; S.ob[j + 1] = b; dl[j + 1] = d;
  }
  int lastW[NSTEP] = {}, lastD[5] = {}, lastAux = 0;
  for (int i = 0; i < NOP; ++i) {
    if (S.ty[i] == 1) lastW[S.oa[i]] = i;
    else if (S.ty[i] == 0) lastD[S.oa[i]] = i;
    else lastAux = i;
  }
  int issued = PRO, done = -1;
  S.opBeg[0] = 0; S.opBeg[1] = PRO;
  auto need = [&](int idx) -> int {
    if (idx <= done) return -1;
    int w = issued - 1 - idx;
    if (w < 0) return -1000;
    if (w > 63) w = 63;
    done = issued - 1 - w;
    return w;
  };
  for (int g = 0; g < PD; ++g) {
    int sl = step_slab_last(S.gs[g]);
    int m = lastD[sl]; if (sl > 0 && lastD[sl - 1] > m) m = lastD[sl - 1];
    S.wD[g] = need(m);
  }
  bool seen[NSTEP] = {};
  for (int g = 0; g < NG; ++g) {
    int s = S.gs[g];
    S.tieW[g] = seen[s] ? 0 : 1;
    int m = lastW[s];
    if (g == 0 && mode == 1 && lastAux > m) m = lastAux;
    S.wW[g] = seen[s] ? -1 : need(m);
    seen[s] = true;
    if (g + PD < NG) {
      int sl = step_slab_last(S.gs[g + PD]);
      int md = lastD[sl]; if (sl > 0 && lastD[sl - 1] > md) md = lastD[sl - 1];
      S.wD[g + PD] = need(md);
    }
    if (issued < NOP) {
      bool avail = S.ty[issued] != 1 || g >= 8 * (S.oa[issued] - LEAD);
      if (avail) ++issued;
    }
    S.opBeg[g + 2] = issued;
  }
  S.wAux = mode == 2 ? need(lastAux) : -1;
  return S;
}
constexpr int step_off(int s) {
  if (s < 3) return s * 32;
  if (s < 5) return (36 + (s - 3)) * 32;
  int q = s - 5, p = q / 9, t = q % 9;
  return (1 + 2 * p) * SLAB_B + ((t / 3) * 18 + t % 3) * 32;
}
constexpr int step_var(int s) { return s < 3 ? 0 : (s < 5 ? 1 : 2); }
constexpr int slot_off(int q) { return q < 18 ? q * 1024 : 5 * SLAB_B + (q - 18) * 1024; }

template <int N>
DEVI void wait_vm() { asm volatile("s_waitcnt vmcnt(%0)" ::"i"(N) : "memory"); }
template <int N>
DEVI void wait_vm5(u32x4 (&f)[5]) {
  if constexpr (N >= 0)
    asm volatile("s_waitcnt vmcnt(%5)" : "+v"(f[0]), "+v"(f[1]), "+v"(f[2]), "+v"(f[3]), "+v"(f[4]) : "i"(N));
  else
    asm volatile("" : "+v"(f[0]), "+v"(f[1]), "+v"(f[2]), "+v"(f[3]), "+v"(f[4]));
}
DEVI void dma16(unsigned m0v, unsigned vo, __amdgpu_buffer_rsrc_t rs, unsigned so) {
  asm volatile("s_mov_b32 m0, %0\n\ts_nop 0\n\tbuffer_load_dwordx4 %1, %2, %3 offen lds" ::"s"(m0v), "v"(vo), "s"(rs), "s"(so) : "memory");
}
template <int IMM>
DEVI void bload16(u32x4& d, unsigned vo, __amdgpu_buffer_rsrc_t rs, unsigned so) {
  asm volatile("buffer_load_dwordx4 %0, %1, %2, %3 offen offset:%4" : "=v"(d) : "v"(vo), "s"(rs), "s"(so), "i"(IMM));
}

template <int MODE>
__global__ __launch_bounds__(256, 1) void conv_k(const char* __restrict__ act, const char* __restrict__ wt,
                                                 const char* __restrict__ aux, char* __restrict__ outp,
                                                 float* __restrict__ stats) {
  __shared__ __attribute__((aligned(1024))) char lds[4 * WREG_B];
  static constexpr Sched S = make_sched(MODE);
  const int bid = blockIdx.x, tid = threadIdx.x, lane = tid & 63;
  const int w = __builtin_amdgcn_readfirstlane(tid >> 6);
  const int xcd = bid & 7, idx = bid >> 3;
  const int b = xcd >> 2, ty = 2 * (xcd & 3) + (idx >> 4), tx = (idx >> 2) & 3, ns = idx & 3;
  const int y0 = ty * 8, x0 = tx * 16;
  const unsigned ldsb = (unsigned)(size_t)(LDS_AS char*)lds;
  const unsigned wreg = ldsb + w * WREG_B;

  __amdgpu_buffer_rsrc_t rsA = __builtin_amdgcn_make_buffer_rsrc((void*)act, (short)0, ACT_B, 0x00020000);
  __amdgpu_buffer_rsrc_t rsW = __builtin_amdgcn_make_buffer_rsrc((void*)(wt + (size_t)(ns * 4 + w) * WQ_B), (short)0, WQ_B, 0x00020000);
  __amdgpu_buffer_rsrc_t rsX = __builtin_amdgcn_make_buffer_rsrc((void*)aux, (short)0, MODE == 1 ? 4 * 2 * 320 * 4 : ACT_B, 0x00020000);

  unsigned dvo[6];
#pragma unroll
  for (int j = 0; j < 6; ++j) {
    const int u = 64 * j + lane, p = u >> 1;
    const int row = (p * 3641) >> 16, col = p - row * 18;
    const int gy = y0 - 1 + row, gx = x0 - 1 + col;
    const bool ok = p < 180 && (unsigned)gy < 64u && (unsigned)gx < 64u;
    dvo[j] = ok ? (unsigned)((gy * 64 + gx) * 32 + (u & 1) * 16) : 0x80000000u;
  }
  const unsigned dso = (unsigned)((b * 20 + w * 5) * PLANE_B);
  const unsigned wvo = lane * 16;
  const int kg = lane >> 4, l15 = lane & 15;
  const unsigned avo = MODE == 1 ? (unsigned)(((w * 2 + b) * NCH + ns * 80 + kg * 4) * 4)
                                 : (unsigned)((((b * NCH + ns * 80 + l15) * 64 + y0 + 2 * w + (kg & 1)) * 64 + x0 + (kg >> 1) * 8) * 2);
  unsigned vb[3][4];
#pragma unroll
  for (int k = 0; k < 4; ++k) {
    const unsigned base = wreg + l15 * 32 + (kg & 1) * 16 + ((w + 1 + k) & 3) * 1152;
    vb[0][k] = base + (kg >> 1) * 576;
    vb[1][k] = base + (kg >> 1) * 32;
    vb[2][k] = base + (kg >> 1) * SLAB_B;
  }

  u32x4 Wf[NSTEP][5];
  u32x4 Ax[5];
  u32x4 Pf[PD + 1];
  f32x4 acc[8][5];
  f32x4 part[3][10];

  auto issue_op = [&](auto K) {
    constexpr int k = K, oa = S.oa[k], ob = S.ob[k];
    if constexpr (S.ty[k] == 0) dma16(wreg + oa * SLAB_B + ob * 1024, dvo[ob], rsA, dso + oa * PLANE_B);
    else if constexpr (S.ty[k] == 1) bload16<((oa * 5 + ob) & 3) * 1024>(Wf[oa][ob], wvo, rsW, ((oa * 5 + ob) >> 2) * 4096);
    else if constexpr (MODE == 1) bload16<oa * 64>(Ax[oa], avo, rsX, 0);
    else bload16<0>(Ax[oa], avo, rsX, oa * 16 * NPX * 2);
  };
  auto read_p = [&](auto G) {
    constexpr int g = G, s = S.gs[g], i = S.gr[g];
    if constexpr (S.wD[g] >= 0) wait_vm<S.wD[g]>();
    static_assert(S.wD[g] > -1000, "sched");
    Pf[g % (PD + 1)] = *(const LDS_AS u32x4*)(size_t)(vb[step_var(s)][i >> 1] + step_off(s) + (i & 1) * 576);
  };

  asm volatile("s_nop 4" ::: "memory");
  sfor<PRO>([&](auto K) { issue_op(K); });
  sfor<PD>([&](auto G) { read_p(G); });
  __builtin_amdgcn_sched_barrier(0);

  const unsigned wrl = wreg + lane * 16;
  const unsigned wsp = wrl + (w < 2 ? 3456 : 0);
  unsigned rdb[3];
#pragma unroll
  for (int d = 1; d <= 3; ++d) rdb[d - 1] = ldsb + ((w - d) & 3) * WREG_B + lane * 16;
  const unsigned rsp = rdb[2] + (((w - 3) & 3) < 2 ? 3456 : 0);

  sfor<NG>([&](auto G) {
    constexpr int g = G, s = S.gs[g], i = S.gr[g];
    static_assert(S.wW[g] > -1000, "sched");
    if constexpr (S.tieW[g]) {
      wait_vm5<S.wW[g]>(Wf[s]);
      if constexpr (g == 0 && MODE == 1) wait_vm5<-1>(Ax);
    }
    constexpr int ft = g >= GF ? (g - GF) % TF : -1;
    if constexpr (g >= GF && ft == 0 && (i == 5 || i == 7)) {
      asm volatile("s_waitcnt lgkmcnt(0)\n\ts_barrier" ::: "memory");
    }
    const bf16x8 pf = __builtin_bit_cast(bf16x8, Pf[g % (PD + 1)]);
    sfor<5>([&](auto N) {
      constexpr int n = N;
      const bf16x8 wf = __builtin_bit_cast(bf16x8, Wf[s][n]);
      f32x4 cin;
      if constexpr (s == 0) {
        if constexpr (MODE == 1) cin = __builtin_bit_cast(f32x4, Ax[n]);
        else cin = (f32x4){0.f, 0.f, 0.f, 0.f};
      } else cin = acc[i][n];
      if constexpr (MODE == 1) acc[i][n] = __builtin_amdgcn_mfma_f32_16x16x32_bf16(wf, pf, cin, 0, 0, 0);
      else acc[i][n] = __builtin_amdgcn_mfma_f32_16x16x32_bf16(pf, wf, cin, 0, 0, 0);
      __builtin_amdgcn_sched_barrier(0);
      if constexpr (n == 0 && g + PD < NG) read_p(std::integral_constant<int, (g + PD < NG ? g + PD : 0)>{});
      if constexpr (n == 1)
        sfor<S.opBeg[g + 2] - S.opBeg[g + 1]>([&](auto K) { issue_op(std::integral_constant<int, S.opBeg[g + 1] + K>{}); });
      if constexpr (g >= GF && i >= 1 && i <= 6 && n >= 2) {
        sfor<5>([&](auto M) {
          constexpr int m = M, q = (i - 1) * 5 + m;
          constexpr int pos = m - (((m * TF) / 5) * 5 + TF - 1) / TF;
          if constexpr ((m * TF) / 5 == ft && 2 + pos == n) {
            if constexpr (q < 28) *(LDS_AS f32x4*)(size_t)(wrl + slot_off(q)) = acc[i - 1][m];
            else *(LDS_AS f32x4*)(size_t)(wsp + (q - 25) * SLAB_B) = acc[i - 1][m];
          }
        });
      }
      if constexpr (g >= GF && i >= 5 && n >= 2) {
        constexpr int gi = (i - 5) * TF + ft;
        sfor<30>([&](auto Q) {
          constexpr int q = Q;
          constexpr int tg = (q * TF) / 10;
          constexpr int pos = q - (tg * 10 + TF - 1) / TF;
          if constexpr (tg == gi && 2 + (pos % 3) == n) {
            if constexpr (q < 28) part[q / 10][q % 10] = *(const LDS_AS f32x4*)(size_t)(rdb[q / 10] + slot_off(q));
            else part[2][q % 10] = *(const LDS_AS f32x4*)(size_t)(rsp + (q - 25) * SLAB_B);
          }
        });
      }
      __builtin_amdgcn_sched_barrier(0);
    });
  });

  f32x4 v[2][5];
#pragma unroll
  for (int e = 0; e < 2; ++e)
#pragma unroll
    for (int n = 0; n < 5; ++n)
      v[e][n] = (acc[6 + e][n] + part[0][e * 5 + n]) + (part[1][e * 5 + n] + part[2][e * 5 + n]);

  if constexpr (MODE == 1) {
    char* dst = outp + (size_t)((b * 20 + ns * 5) * PLANE_B) + ((y0 + 2 * w + (kg & 1)) * 64 + x0 + l15) * 32 + (kg >> 1) * 16;
    float s = 0.f;
#pragma unroll
    for (int n = 0; n < 5; ++n) {
      unsigned a0 = pk_bf16(v[0][n][0], v[0][n][1]), a1 = pk_bf16(v[0][n][2], v[0][n][3]);
      unsigned b0 = pk_bf16(v[1][n][0], v[1][n][1]), b1 = pk_bf16(v[1][n][2], v[1][n][3]);
      u32x2 s0 = __builtin_amdgcn_permlane16_swap(a0, b0, false, false);
      u32x2 s1 = __builtin_amdgcn_permlane16_swap(a1, b1, false, false);
      *(u32x4*)(dst + (size_t)n * PLANE_B) = (u32x4){s0.x, s1.x, s0.y, s1.y};
#pragma unroll
      for (int e = 0; e < 2; ++e) s += (v[e][n][0] + v[e][n][1]) + (v[e][n][2] + v[e][n][3]);
    }
    const float mean = wave_sum(s) * (1.0f / 2560.0f);
    float m2 = 0.f;
#pragma unroll
    for (int n = 0; n < 5; ++n)
#pragma unroll
      for (int e = 0; e < 2; ++e)
#pragma unroll
        for (int j = 0; j < 4; ++j) { float d = v[e][n][j] - mean; m2 += d * d; }
    m2 = wave_sum(m2);
    if (lane == 0) *(f32x2*)(stats + ((b * 4 + ns) * 128 + (ty * 4 + tx) * 4 + w) * 2) = (f32x2){mean, m2};
  } else {
    wait_vm5<S.wAux>(Ax);
    char* dst = outp + avo;
#pragma unroll
    for (int n = 0; n < 5; ++n) {
      float lo[4], hi[4];
#pragma unroll
      for (int j = 0; j < 4; ++j) {
        u32x2 t = __builtin_amdgcn_permlane16_swap(__float_as_uint(v[0][n][j]), __float_as_uint(v[1][n][j]), false, false);
        lo[j] = __uint_as_float(t.x); hi[j] = __uint_as_float(t.y);
      }
      u32x4 o;
      o.x = pk_bf16(lo[0] + bf_lo(Ax[n].x), lo[1] + bf_hi(Ax[n].x));
      o.y = pk_bf16(lo[2] + bf_lo(Ax[n].y), lo[3] + bf_hi(Ax[n].y));
      o.z = pk_bf16(hi[0] + bf_lo(Ax[n].z), hi[1] + bf_hi(Ax[n].z));
      o.w = pk_bf16(hi[2] + bf_lo(Ax[n].w), hi[3] + bf_hi(Ax[n].w));
      *(u32x4*)(dst + (size_t)n * (16 * NPX * 2)) = o;
    }
  }
}

extern "C" void kernel_launch(void* const* d_in, const int* in_sizes, int n_in, void* d_out, int out_size, void* d_ws,
                              size_t ws_size, hipStream_t stream) {
  const uint16_t* x = (const uint16_t*)d_in[0];
  const uint16_t* temb = (const uint16_t*)d_in[1];
  const uint16_t* g1 = (const uint16_t*)d_in[2];
  const uint16_t* be1 = (const uint16_t*)d_in[3];
  const uint16_t* w1 = (const uint16_t*)d_in[4];
  const uint16_t* wemb = (const uint16_t*)d_in[5];
  const uint16_t* g2 = (const uint16_t*)d_in[6];
  const uint16_t* be2 = (const uint16_t*)d_in[7];
  const uint16_t* w2 = (const uint16_t*)d_in[8];
  char* ws = (char*)d_ws;
  prep_k<<<376, 320, 0, stream>>>(x, temb, w1, wemb, w2, ws);
  act1_k<<<256, 320, 0, stream>>>(x, g1, be1, ws);
  conv_k<1><<<256, 256, 0, stream>>>(ws + OFF_ACT1, ws + OFF_WT1, ws + OFF_EMB, ws + OFF_ACT2, (float*)(ws + OFF_ST2));
  act2_k<<<256, 320, 0, stream>>>(g2, be2, ws);
  conv_k<2><<<256, 256, 0, stream>>>(ws + OFF_ACT2, ws + OFF_WT2, (const char*)x, (char*)d_out, (float*)(ws + OFF_ST2));
}
```

```cpp
#include <hip/hip_runtime.h>
#include <hip/hip_bf16.h>
#include <stdint.h>
#include <utility>

typedef __attribute__((ext_vector_type(8))) __bf16 bf16x8;
typedef __attribute__((ext_vector_type(2))) __bf16 bf16x2;
typedef __attribute__((ext_vector_type(4))) float f32x4;
typedef __attribute__((ext_vector_type(2))) float f32x2;
typedef __attribute__((ext_vector_type(4))) unsigned int u32x4;
typedef __attribute__((ext_vector_type(2))) unsigned int u32x2;
#define LDS_AS __attribute__((address_space(3)))
#define DEVI __device__ __forceinline__

constexpr int NCH = 320, NPX = 4096, NEMB = 1152;
constexpr int PLANE_B = NPX * 32;
constexpr int ACT_B = 2 * 20 * PLANE_B;
constexpr int NSTEP = 23;
constexpr int WQ_B = NSTEP * 5 * 1024;
constexpr int WT_B = 16 * WQ_B;
constexpr size_t OFF_ACT1 = 0;
constexpr size_t OFF_ACT2 = OFF_ACT1 + ACT_B;
constexpr size_t OFF_WT1 = OFF_ACT2 + ACT_B;
constexpr size_t OFF_WT2 = OFF_WT1 + WT_B;
constexpr size_t OFF_EMB = OFF_WT2 + WT_B;
constexpr size_t OFF_SYNC = OFF_EMB + 16384;
constexpr size_t SY_EPOCH = 0;
constexpr size_t SY_MB1 = 256;
constexpr size_t SY_MB2 = SY_MB1 + 65536;
constexpr size_t SY_ST1 = SY_MB2 + 65536;
constexpr size_t SY_ST2 = SY_ST1 + 131072;
constexpr size_t SY_FJ = SY_ST2 + 131072;
constexpr int FJ_STR = 2304;
typedef unsigned long long u64;

#define DG(i)

template <typename F, int... I>
DEVI void sfor_impl(F&& f, std::integer_sequence<int, I...>) { (f(std::integral_constant<int, I>{}), ...); }
template <int N, typename F>
DEVI void sfor(F&& f) { sfor_impl(f, std::make_integer_sequence<int, N>{}); }

DEVI float bf_lo(unsigned v) { return __uint_as_float(v << 16); }
DEVI float bf_hi(unsigned v) { return __uint_as_float(v & 0xffff0000u); }
DEVI unsigned pk_bf16(float a, float b) {
  bf16x2 r = __builtin_convertvector((f32x2){a, b}, bf16x2);
  return __builtin_bit_cast(unsigned, r);
}
DEVI float silu_f(float y) {
  float e = __builtin_amdgcn_exp2f(y * -1.44269504088896341f);
  return y * __builtin_amdgcn_rcpf(1.0f + e);
}
template <int CTRL>
DEVI float dpp_add(float v) {
  return v + __builtin_bit_cast(float, __builtin_amdgcn_update_dpp(0, __builtin_bit_cast(int, v), CTRL, 0xf, 0xf, true));
}
DEVI float wave_sum(float v) {
  v = dpp_add<0xB1>(v);
  v = dpp_add<0x4E>(v);
  v = dpp_add<0x141>(v);
  v = dpp_add<0x140>(v);
  int iv = __builtin_bit_cast(int, v);
  float a = __builtin_bit_cast(float, __builtin_amdgcn_readlane(iv, 0));
  float b = __builtin_bit_cast(float, __builtin_amdgcn_readlane(iv, 16));
  float c = __builtin_bit_cast(float, __builtin_amdgcn_readlane(iv, 32));
  float d = __builtin_bit_cast(float, __builtin_amdgcn_readlane(iv, 48));
  return (a + b) + (c + d);
}

#define P_TF 4
#define P_NPW 5
#define P_PRO 6
#define P_LEAD 7
constexpr int TF = P_TF;
constexpr int PD = 4;
constexpr int LEAD = P_LEAD;
constexpr int NPW = P_NPW;
constexpr int PRO = P_PRO;
constexpr int NG = NSTEP * 8;
constexpr int GF = (NSTEP - TF) * 8;
constexpr int NOPM = 150;
constexpr int SLAB_B = 6144, WREG_B = 40960;
constexpr int RED_OFF = 3 * SLAB_B + 5760;

struct Sched {
  int nop;
  int ty[NOPM], oa[NOPM], ob[NOPM];
  int opBeg[NG + 2];
  int gs[NG], gr[NG];
  int wW[NG], wD[NG + PD], tieW[NG];
  int wAux;
};
constexpr int step_slab_last(int s) { return s < 5 ? 0 : (s < 14 ? 2 : 4); }
constexpr Sched make_sched(int mode) {
  Sched S{};
  int fu[NSTEP] = {};
  for (int g = 0; g < NG; ++g) {
    if (g < GF) { S.gs[g] = g / 8; S.gr[g] = g % 8; }
    else { int k = g - GF; S.gr[g] = k / TF; S.gs[g] = NSTEP - TF + k % TF; }
  }
  for (int s = 0; s < NSTEP; ++s) fu[s] = s < NSTEP - TF ? 8 * s : GF + (s - (NSTEP - TF));
  int dl[NOPM] = {};
  int n = 0;
  for (int p = 0; p < 6; ++p) { S.ty[n] = 0; S.oa[n] = 0; S.ob[n] = p; dl[n] = -10; ++n; }
  if (mode == 2)
    for (int k = 0; k < 5; ++k) { S.ty[n] = 2; S.oa[n] = k; S.ob[n] = 0; dl[n] = GF + 8; ++n; }
  for (int s = NPW; s < NSTEP; ++s)
    for (int k = 0; k < 5; ++k) { S.ty[n] = 1; S.oa[n] = s; S.ob[n] = k; dl[n] = fu[s]; ++n; }
  for (int sl = 1; sl < 5; ++sl)
    for (int p = 0; p < 6; ++p) { S.ty[n] = 0; S.oa[n] = sl; S.ob[n] = p; dl[n] = (sl < 3 ? fu[5] : fu[14]) - PD - 12; ++n; }
  const int NOP = n;
  S.nop = n;
  for (int i = 1; i < NOP; ++i) {
    int t = S.ty[i], a = S.oa[i], b = S.ob[i], d = dl[i], j = i - 1;
    while (j >= 0 && dl[j] > d) { S.ty[j + 1] = S.ty[j]; S.oa[j + 1] = S.oa[j]; S.ob[j + 1] = S.ob[j]; dl[j + 1] = dl[j]; --j; }
    S.ty[j + 1] = t; S.oa[j + 1] = a; S.ob[j + 1] = b; dl[j + 1] = d;
  }
  int lastW[NSTEP] = {}, lastD[5] = {}, lastAux = -1;
  for (int s = 0; s < NSTEP; ++s) lastW[s] = -1;
  for (int i = 0; i < NOP; ++i) {
    if (S.ty[i] == 1) lastW[S.oa[i]] = i;
    else if (S.ty[i] == 0) lastD[S.oa[i]] = i;
    else lastAux = i;
  }
  int issued = PRO, done = -1;
  S.opBeg[0] = 0; S.opBeg[1] = PRO;
  auto need = [&](int idx) -> int {
    if (idx <= done) return -1;
    int w = issued - 1 - idx;
    if (w < 0) return -1000;
    if (w > 63) w = 63;
    done = issued - 1 - w;
    return w;
  };
  for (int g = 0; g < PD; ++g) {
    int sl = step_slab_last(S.gs[g]);
    int m = lastD[sl]; if (sl > 0 && lastD[sl - 1] > m) m = lastD[sl - 1];
    S.wD[g] = need(m);
  }
  bool seen[NSTEP] = {};
  for (int g = 0; g < NG; ++g) {
    int s = S.gs[g];
    S.tieW[g] = (seen[s] || s < NPW) ? 0 : 1;
    S.wW[g] = seen[s] ? -1 : need(lastW[s]);
    seen[s] = true;
    if (g + PD < NG) {
      int sl = step_slab_last(S.gs[g + PD]);
      int md = lastD[sl]; if (sl > 0 && lastD[sl - 1] > md) md = lastD[sl - 1];
      S.wD[g + PD] = need(md);
    }
    if (issued < NOP) {
      bool avail = S.ty[issued] != 1 || g >= 8 * (S.oa[issued] - LEAD);
      if (avail) ++issued;
    }
    S.opBeg[g + 2] = issued;
  }
  S.wAux = mode == 2 ? need(lastAux) : -1;
  return S;
}
constexpr int step_off(int s) {
  if (s < 3) return s * 32;
  if (s < 5) return (36 + (s - 3)) * 32;
  int q = s - 5, p = q / 9, t = q % 9;
  return (1 + 2 * p) * SLAB_B + ((t / 3) * 18 + t % 3) * 32;
}
constexpr int step_var(int s) { return s < 3 ? 0 : (s < 5 ? 1 : 2); }
constexpr int slot_off(int q) { return q < 18 ? q * 1024 : 5 * SLAB_B + (q - 18) * 1024; }

template <int N>
DEVI void wait_vm() { asm volatile("s_waitcnt vmcnt(%0)" ::"i"(N) : "memory"); }
template <int N>
DEVI void wait_vm5(u32x4 (&f)[5]) {
  if constexpr (N >= 0)
    asm volatile("s_waitcnt vmcnt(%5)" : "+v"(f[0]), "+v"(f[1]), "+v"(f[2]), "+v"(f[3]), "+v"(f[4]) : "i"(N));
  else
    asm volatile("" : "+v"(f[0]), "+v"(f[1]), "+v"(f[2]), "+v"(f[3]), "+v"(f[4]));
}
DEVI void dma16(unsigned m0v, unsigned vo, __amdgpu_buffer_rsrc_t rs, unsigned so) {
  asm volatile("s_mov_b32 m0, %0\n\ts_nop 0\n\tbuffer_load_dwordx4 %1, %2, %3 offen lds" ::"s"(m0v), "v"(vo), "s"(rs), "s"(so) : "memory");
}
template <int IMM>
DEVI void bload16(u32x4& d, unsigned vo, __amdgpu_buffer_rsrc_t rs, unsigned so) {
  asm volatile("buffer_load_dwordx4 %0, %1, %2, %3 offen offset:%4" : "=v"(d) : "v"(vo), "s"(rs), "s"(so), "i"(IMM));
}
DEVI void wg_barrier() { asm volatile("s_waitcnt lgkmcnt(0)\n\ts_barrier" ::: "memory"); }
DEVI __amdgpu_buffer_rsrc_t mk_rsrc(const void* p, unsigned bytes) {
  return __builtin_amdgcn_make_buffer_rsrc((void*)p, (short)0, bytes, 0x00020000);
}
DEVI void st16_dev(u32x4 v, __amdgpu_buffer_rsrc_t rs, unsigned vo, unsigned so) {
  __builtin_amdgcn_raw_buffer_store_b128(v, rs, vo, so, 16);
}
#define ACT_AUX 16
DEVI void st16_act(u32x4 v, __amdgpu_buffer_rsrc_t rs, unsigned vo, unsigned so) {
  __builtin_amdgcn_raw_buffer_store_b128(v, rs, vo, so, ACT_AUX);
}
DEVI u64 ld_dev(const char* p) { return __hip_atomic_load((const u64*)p, __ATOMIC_RELAXED, __HIP_MEMORY_SCOPE_AGENT); }
DEVI void st_dev(char* p, u64 v) { __hip_atomic_store((u64*)p, v, __ATOMIC_RELAXED, __HIP_MEMORY_SCOPE_AGENT); }
constexpr int SPIN_MAX = 1 << 15;
DEVI void wait_words(const char* p, bool on, u64 tag, u64 first) {
  u64 v = first;
#pragma unroll 1
  for (int it = 0; it < SPIN_MAX; ++it) {
    if (__builtin_amdgcn_ballot_w64(on && v != tag) == 0) break;
    __builtin_amdgcn_s_sleep(1);
    v = ld_dev(p);
  }
}
DEVI void acquire_dev() {
  asm volatile("" ::: "memory");
}
DEVI void release_dev() {
  wait_vm<0>();
}

struct Geo {
  unsigned dvo[6], vb[3][4], wreg, ldsb, wvo, avo;
  int w, lane;
};

template <int MODE>
DEVI void conv_main(const Geo& G_, __amdgpu_buffer_rsrc_t rsA, unsigned dso, __amdgpu_buffer_rsrc_t rsW,
                    __amdgpu_buffer_rsrc_t rsX, u32x4 (&Wp)[NPW][5], u32x4 (&Ax)[5], f32x4 (&v)[2][5]) {
  static constexpr Sched S = make_sched(MODE);
  const unsigned wreg = G_.wreg, ldsb = G_.ldsb;
  const int w = G_.w, lane = G_.lane;
  u32x4 Wf[NSTEP][5];
  u32x4 Pf[PD + 1];
  f32x4 acc[8][5];
  f32x4 part[3][10];
  sfor<NPW>([&](auto T) { sfor<5>([&](auto N) { Wf[T][N] = Wp[T][N]; }); });

  auto issue_op = [&](auto K) {
    constexpr int k = K, oa = S.oa[k], ob = S.ob[k];
    if constexpr (S.ty[k] == 0) dma16(wreg + oa * SLAB_B + ob * 1024, G_.dvo[ob], rsA, dso + oa * PLANE_B);
    else if constexpr (S.ty[k] == 1) bload16<((oa * 5 + ob) & 3) * 1024>(Wf[oa][ob], G_.wvo, rsW, ((oa * 5 + ob) >> 2) * 4096);
    else bload16<0>(Ax[oa], G_.avo, rsX, oa * 16 * NPX * 2);
  };
  auto read_p = [&](auto G) {
    constexpr int g = G, s = S.gs[g], i = S.gr[g];
    if constexpr (S.wD[g] >= 0) wait_vm<S.wD[g]>();
    static_assert(S.wD[g] > -1000, "sched");
    Pf[g % (PD + 1)] = *(const LDS_AS u32x4*)(size_t)(G_.vb[step_var(s)][i >> 1] + step_off(s) + (i & 1) * 576);
  };

  __builtin_amdgcn_sched_barrier(0);
  asm volatile("s_nop 4" ::: "memory");
  sfor<PRO>([&](auto K) { issue_op(K); });
  sfor<PD>([&](auto G) { read_p(G); });
  __builtin_amdgcn_sched_barrier(0);

  const unsigned wrl = wreg + lane * 16;
  const unsigned wsp = wrl + (w < 2 ? 3456 : 0);
  unsigned rdb[3];
#pragma unroll
  for (int d = 1; d <= 3; ++d) rdb[d - 1] = ldsb + ((w - d) & 3) * WREG_B + lane * 16;
  const unsigned rsp = rdb[2] + (((w - 3) & 3) < 2 ? 3456 : 0);

  sfor<NG>([&](auto G) {
    constexpr int g = G, s = S.gs[g], i = S.gr[g];
    static_assert(S.wW[g] > -1000, "sched");
    if constexpr (S.tieW[g]) wait_vm5<S.wW[g]>(Wf[s]);
    constexpr int ft = g >= GF ? (g - GF) % TF : -1;
    if constexpr (g >= GF && ft == 0 && (i == 5 || i == 7)) wg_barrier();
    const bf16x8 pf = __builtin_bit_cast(bf16x8, Pf[g % (PD + 1)]);
    sfor<5>([&](auto N) {
      constexpr int n = N;
      const bf16x8 wf = __builtin_bit_cast(bf16x8, Wf[s][n]);
      f32x4 cin;
      if constexpr (s == 0) {
        if constexpr (MODE == 1) cin = __builtin_bit_cast(f32x4, Ax[n]);
        else cin = (f32x4){0.f, 0.f, 0.f, 0.f};
      } else cin = acc[i][n];
      if constexpr (MODE == 1) acc[i][n] = __builtin_amdgcn_mfma_f32_16x16x32_bf16(wf, pf, cin, 0, 0, 0);
      else acc[i][n] = __builtin_amdgcn_mfma_f32_16x16x32_bf16(pf, wf, cin, 0, 0, 0);
      __builtin_amdgcn_sched_barrier(0);
      if constexpr (n == 0 && g + PD < NG) read_p(std::integral_constant<int, (g + PD < NG ? g + PD : 0)>{});
      if constexpr (n == 1)
        sfor<S.opBeg[g + 2] - S.opBeg[g + 1]>([&](auto K) { issue_op(std::integral_constant<int, S.opBeg[g + 1] + K>{}); });
      if constexpr (g >= GF && i >= 1 && i <= 6 && n >= 2) {
        sfor<5>([&](auto M) {
          constexpr int m = M, q = (i - 1) * 5 + m;
          constexpr int pos = m - (((m * TF) / 5) * 5 + TF - 1) / TF;
          if constexpr ((m * TF) / 5 == ft && 2 + pos == n) {
            if constexpr (q < 28) *(LDS_AS f32x4*)(size_t)(wrl + slot_off(q)) = acc[i - 1][m];
            else *(LDS_AS f32x4*)(size_t)(wsp + (q - 25) * SLAB_B) = acc[i - 1][m];
          }
        });
      }
      if constexpr (g >= GF && i >= 5 && n >= 2) {
        constexpr int gi = (i - 5) * TF + ft;
        sfor<30>([&](auto Q) {
          constexpr int q = Q;
          constexpr int tg = (q * TF) / 10;
          constexpr int pos = q - (tg * 10 + TF - 1) / TF;
          if constexpr (tg == gi && 2 + (pos % 3) == n) {
            if constexpr (q < 28) part[q / 10][q % 10] = *(const LDS_AS f32x4*)(size_t)(rdb[q / 10] + slot_off(q));
            else part[2][q % 10] = *(const LDS_AS f32x4*)(size_t)(rsp + (q - 25) * SLAB_B);
          }
        });
      }
      __builtin_amdgcn_sched_barrier(0);
    });
  });
#pragma unroll
  for (int e = 0; e < 2; ++e)
#pragma unroll
    for (int n = 0; n < 5; ++n)
      v[e][n] = (acc[6 + e][n] + part[0][e * 5 + n]) + (part[1][e * 5 + n] + part[2][e * 5 + n]);
  if constexpr (MODE == 2) wait_vm5<S.wAux>(Ax);
}

DEVI void publish_stats(unsigned ldsb, char* ent, u64 tag, int lane) {
  f32x2 e[4];
#pragma unroll
  for (int k = 0; k < 4; ++k) e[k] = *(const LDS_AS f32x2*)(size_t)(ldsb + k * WREG_B + RED_OFF);
  const float mean = ((e[0].x + e[1].x) + (e[2].x + e[3].x)) * 0.25f;
  float m2 = 0.f;
#pragma unroll
  for (int k = 0; k < 4; ++k) { float d = e[k].x - mean; m2 += e[k].y + 2560.0f * d * d; }
  const unsigned t32 = (lane & 1) ? (unsigned)(tag >> 32) : (unsigned)tag;
  st_dev(ent + (lane >> 1) * 512 + (lane & 1) * 8, (u64)__float_as_uint((lane & 1) ? m2 : mean) | (u64)t32 << 32);
}
DEVI void gather_stats(unsigned ldsb, const char* grp, u64 tag, int lane) {
  const unsigned t32 = (lane & 1) ? (unsigned)(tag >> 32) : (unsigned)tag;
  u64 v = ld_dev(grp + lane * 8);
#pragma unroll 1
  for (int it = 0; it < SPIN_MAX; ++it) {
    if (__builtin_amdgcn_ballot_w64((unsigned)(v >> 32) != t32) == 0) break;
    __builtin_amdgcn_s_sleep(1);
    v = ld_dev(grp + lane * 8);
  }
  const float f = __uint_as_float((unsigned)v);
  const float mu = wave_sum((lane & 1) ? 0.f : f) * (1.0f / 32.0f);
  const float d = f - mu;
  const float var = wave_sum((lane & 1) ? f : 10240.0f * d * d) * (1.0f / 327680.0f);
  const float rstd = 1.0f / sqrtf(var + 1e-5f);
  if (lane == 0) *(LDS_AS f32x2*)(size_t)(ldsb + RED_OFF + 8) = (f32x2){mu, rstd};
}

__global__ __launch_bounds__(256, 1) void resblock_k(const uint16_t* __restrict__ x, const uint16_t* __restrict__ temb,
                                                     const uint16_t* __restrict__ g1, const uint16_t* __restrict__ be1,
                                                     const uint16_t* __restrict__ w1, const uint16_t* __restrict__ wemb,
                                                     const uint16_t* __restrict__ g2, const uint16_t* __restrict__ be2,
                                                     const uint16_t* __restrict__ w2, char* __restrict__ outp,
                                                     char* __restrict__ ws) {
  __shared__ __attribute__((aligned(1024))) char lds[4 * WREG_B];
  DG(0);
  const int bid = blockIdx.x, tid = threadIdx.x, lane = tid & 63;
  const int w = __builtin_amdgcn_readfirstlane(tid >> 6);
  const int xcd = bid & 7, idx = bid >> 3;
  const int b = xcd >> 2, ty = 2 * (xcd & 3) + (idx >> 4), tx = (idx >> 2) & 3, ns = idx & 3;
  const int y0 = ty * 8, x0 = tx * 16;
  const int kg = lane >> 4, l15 = lane & 15;
  char* sy = ws + OFF_SYNC;

  const int rp = 8 * (xcd & 3) + (idx >> 2);
  const unsigned* xs = (const unsigned*)(x + (size_t)(b * NCH + ns * 80 + kg * 20) * NPX + rp * 128 + w * 32) + l15;
  unsigned xv[20];
#pragma unroll
  for (int i = 0; i < 20; ++i) xv[i] = xs[i * (NPX / 2)];
  const u64 epoch = ld_dev(sy + SY_EPOCH);
  u32x2 gv[5], bv[5];
#pragma unroll
  for (int q = 0; q < 5; ++q) {
    gv[q] = *(const u32x2*)(g1 + ns * 80 + kg * 20 + q * 4);
    bv[q] = *(const u32x2*)(be1 + ns * 80 + kg * 20 + q * 4);
  }
  const bool jw = (w == 1 || w == 2) && bid < 200, je = w == 3 && bid >= 176;
  const int ot = bid / 10, oc = (bid % 10) * 4 + kg;
  const int ekq = (bid - 176) / 20, ecg = (bid - 176) % 20;
  u32x4 jb[9];
  unsigned short tv[9];
  {
    const __amdgpu_buffer_rsrc_t rj = mk_rsrc(je ? wemb : (w == 1 ? w1 : w2), jw ? 320 * 2880 * 2 : (je ? NEMB * NCH * 2 : 0));
    const __amdgpu_buffer_rsrc_t rt_ = mk_rsrc(temb, je ? 2 * NEMB * 2 : 0);
    const unsigned jo = jw ? ((ot * 16 + l15) * 2880 + oc * 72) * 2 : ((ekq * 288 + (lane >> 1)) * NCH + ecg * 16 + (lane & 1) * 8) * 2;
    const unsigned js = jw ? 16 : 32 * NCH * 2;
#pragma unroll
    for (int k = 0; k < 9; ++k) jb[k] = __builtin_amdgcn_raw_buffer_load_b128(rj, jo, k * js, 0);
#pragma unroll
    for (int k = 0; k < 9; ++k)
      tv[k] = __builtin_amdgcn_raw_buffer_load_b16(rt_, ((lane & 1) * NEMB + ekq * 288 + (lane >> 1) + 32 * k) * 2, 0, 0);
  }
  const __amdgpu_buffer_rsrc_t rsS = mk_rsrc(ws, (unsigned)OFF_SYNC);
  const unsigned dst1 = (unsigned)OFF_ACT1 + (b * 20 + ns * 5) * PLANE_B + (rp * 128 + w * 32) * 32 + lane * 16;
  const unsigned dst2 = (unsigned)OFF_ACT2 + (b * 20 + ns * 5) * PLANE_B + ((y0 + 2 * w + (kg & 1)) * 64 + x0 + l15) * 32 + (kg >> 1) * 16;

  Geo G;
  G.w = w; G.lane = lane;
  G.ldsb = (unsigned)(size_t)(LDS_AS char*)lds;
  G.wreg = G.ldsb + w * WREG_B;
  G.wvo = lane * 16;
  G.avo = (unsigned)((((b * NCH + ns * 80 + l15) * 64 + y0 + 2 * w + (kg & 1)) * 64 + x0 + (kg >> 1) * 8) * 2);
#pragma unroll
  for (int j = 0; j < 6; ++j) {
    const int u = 64 * j + lane, p = u >> 1;
    const int row = (p * 3641) >> 16, col = p - row * 18;
    const int gy = y0 - 1 + row, gx = x0 - 1 + col;
    const bool ok = p < 180 && (unsigned)gy < 64u && (unsigned)gx < 64u;
    G.dvo[j] = ok ? (unsigned)((gy * 64 + gx) * 32 + (u & 1) * 16) : 0x80000000u;
  }
#pragma unroll
  for (int k = 0; k < 4; ++k) {
    const unsigned base = G.wreg + l15 * 32 + (kg & 1) * 16 + ((w + 1 + k) & 3) * 1152;
    G.vb[0][k] = base + (kg >> 1) * 576;
    G.vb[1][k] = base + (kg >> 1) * 32;
    G.vb[2][k] = base + (kg >> 1) * SLAB_B;
  }
  const unsigned ldsb = G.ldsb, wreg = G.wreg;

  {
    float s = 0.f;
#pragma unroll
    for (int i = 0; i < 20; ++i) s += bf_lo(xv[i]) + bf_hi(xv[i]);
    const float mean = wave_sum(s) * (1.0f / 2560.0f);
    float m2 = 0.f;
#pragma unroll
    for (int i = 0; i < 20; ++i) {
      float a = bf_lo(xv[i]) - mean, c = bf_hi(xv[i]) - mean;
      m2 += a * a + c * c;
    }
    m2 = wave_sum(m2);
    if (lane == 0) *(LDS_AS f32x2*)(size_t)(wreg + RED_OFF) = (f32x2){mean, m2};
  }
  const u64 tag = (u64)__builtin_amdgcn_readfirstlane((unsigned)epoch) +
                  ((u64)__builtin_amdgcn_readfirstlane((unsigned)(epoch >> 32)) << 32) + 0x9E3779B97F4A7C15ull;
  DG(1);
  wg_barrier();
  if (w == 0) {
    publish_stats(ldsb, sy + SY_ST1 + (b * 4 + ns) * 16384 + rp * 16, tag, lane);
    gather_stats(ldsb, sy + SY_ST1 + (b * 4 + ns) * 16384 + rp * 512, tag, lane);
  } else if (jw) {
    unsigned in[36];
#pragma unroll
    for (int k = 0; k < 9; ++k) { in[4 * k] = jb[k].x; in[4 * k + 1] = jb[k].y; in[4 * k + 2] = jb[k].z; in[4 * k + 3] = jb[k].w; }
    const int kq = oc / 10, rr = oc % 10, slab = rr >> 1, u = rr & 1;
    const unsigned dst = (unsigned)(w == 1 ? OFF_WT1 : OFF_WT2) + ((ot / 5) * 4 + kq) * WQ_B + (ot % 5) * 1024 + l15 * 16 + u * 256;
    const bool single = slab == 0;
    const int pbase = (5 + 9 * ((slab - 1) >> 1)) * 5120 + ((slab - 1) & 1) * 512;
    sfor<9>([&](auto T) {
      constexpr int t = T, dy = t / 3, dx = t % 3;
      constexpr int sstep = dy < 2 ? dx : (dx == 0 ? 3 : 4);
      constexpr int shi = dy < 2 ? dy : (dx == 2 ? 1 : 0);
      u32x4 ov;
      sfor<4>([&](auto Q) {
        constexpr int q = Q, e0 = 9 * (2 * q) + t, e1 = 9 * (2 * q + 1) + t;
        constexpr unsigned sel = (unsigned)((e0 & 1) * 2) | (unsigned)((e0 & 1) * 2 + 1) << 8 |
                                 (unsigned)(4 + (e1 & 1) * 2) << 16 | (unsigned)(4 + (e1 & 1) * 2 + 1) << 24;
        ov[q] = __builtin_amdgcn_perm(in[e1 >> 1], in[e0 >> 1], sel);
      });
      st16_dev(ov, rsS, dst + (single ? sstep * 5120 + shi * 512 : pbase + t * 5120), 0);
    });
    if (single) st16_dev((u32x4){0u, 0u, 0u, 0u}, rsS, dst + 3 * 5120 + 512, 0);
    release_dev();
    if (lane < 4) st_dev(sy + SY_FJ + ((w - 1) * 4 + lane) * FJ_STR + bid * 8, tag);
  } else if (je) {
    float acc[2][8];
#pragma unroll
    for (int i = 0; i < 8; ++i) acc[0][i] = acc[1][i] = 0.f;
#pragma unroll
    for (int k = 0; k < 9; ++k) {
      const float so = silu_f(bf_lo(tv[k]));
      const float sx = __builtin_bit_cast(float, __builtin_amdgcn_update_dpp(0, __builtin_bit_cast(int, so), 0xB1, 0xf, 0xf, true));
      const float s0 = (lane & 1) ? sx : so, s1 = (lane & 1) ? so : sx;
#pragma unroll
      for (int q = 0; q < 4; ++q) {
        const float a = bf_lo(jb[k][q]), c = bf_hi(jb[k][q]);
        acc[0][2 * q] += s0 * a; acc[0][2 * q + 1] += s0 * c;
        acc[1][2 * q] += s1 * a; acc[1][2 * q + 1] += s1 * c;
      }
    }
    float c8[8];
#pragma unroll
    for (int i = 0; i < 8; ++i) {
      u32x2 t = __builtin_amdgcn_permlane16_swap(__float_as_uint(acc[0][i]), __float_as_uint(acc[1][i]), false, false);
      c8[i] = __uint_as_float(t.x) + __uint_as_float(t.y);
    }
    u32x4 o;
#pragma unroll
    for (int i = 0; i < 4; ++i) {
      u32x2 t = __builtin_amdgcn_permlane32_swap(__float_as_uint(c8[i]), __float_as_uint(c8[i + 4]), false, false);
      float d = __uint_as_float(t.x) + __uint_as_float(t.y);
      d = dpp_add<0x4E>(d); d = dpp_add<0x124>(d); d = dpp_add<0x128>(d);
      o[i] = __float_as_uint(d);
    }
    if (l15 < 2) st16_dev(o, rsS, (unsigned)OFF_EMB + ((ekq * 20 + ecg) * 32 + (kg & 1) * 16 + l15 * 8 + (kg >> 1) * 4) * 4, 0);
    release_dev();
    if (lane < 4) st_dev(sy + SY_FJ + (8 + lane) * FJ_STR + ((ecg / 5) * 20 + ekq * 5 + ecg % 5) * 8, tag);
  }
  DG(2);
  wg_barrier();
  DG(3);

  const bool fjon = w != 0 && lane < (w == 3 ? 20 : 50);
  const char* fjp = sy + SY_FJ + (((w + 3) & 3) * 4 + tx) * FJ_STR + (ns * (w == 3 ? 20 : 50) + lane) * 8;
  u64 fj0 = 0;
  if (w != 0) fj0 = ld_dev(fjp);
  {
    const f32x2 st = *(const LDS_AS f32x2*)(size_t)(ldsb + RED_OFF + 8);
    const unsigned stg = wreg + 5 * SLAB_B;
#pragma unroll
    for (int q = 0; q < 5; ++q) {
      unsigned o[2][2];
#pragma unroll
      for (int h = 0; h < 2; ++h) {
        const float a0 = st.y * bf_lo(gv[q][h]), a1 = st.y * bf_hi(gv[q][h]);
        const float b0 = bf_lo(bv[q][h]) - st.x * a0, b1 = bf_hi(bv[q][h]) - st.x * a1;
        const unsigned u0 = xv[4 * q + 2 * h], u1 = xv[4 * q + 2 * h + 1];
        o[0][h] = pk_bf16(silu_f(bf_lo(u0) * a0 + b0), silu_f(bf_lo(u1) * a1 + b1));
        o[1][h] = pk_bf16(silu_f(bf_hi(u0) * a0 + b0), silu_f(bf_hi(u1) * a1 + b1));
      }
      const int c = kg * 20 + q * 4;
      const unsigned ad = stg + (c >> 4) * 1024 + l15 * 64 + (c & 4) * 2;
#pragma unroll
      for (int j = 0; j < 2; ++j)
        *(LDS_AS u32x2*)(size_t)(ad + (((j * 2 + ((c >> 3) & 1)) ^ (l15 >> 2)) << 4)) = (u32x2){o[j][0], o[j][1]};
    }
    __builtin_amdgcn_wave_barrier();
#pragma unroll
    for (int m = 0; m < 5; ++m) {
      const u32x4 q = *(const LDS_AS u32x4*)(size_t)(stg + m * 1024 + ((lane & ~3) << 4) + (((lane & 3) ^ kg) << 4));
      st16_act(q, rsS, dst1, m * PLANE_B);
    }
  }
  DG(4);
  wait_words(fjp, fjon, tag, fj0);
  release_dev();
  wg_barrier();
  if (tid < 8) {
    const int r3 = rp & 3, up = tid >> 2;
    const int cty = (rp >> 2) + (up ? (r3 == 0 ? -1 : 1) : 0), slot = up ? (r3 == 0 ? 5 : 0) : r3 + 1;
    if (!up || ((r3 == 0 || r3 == 3) && (unsigned)cty < 8u))
      st_dev(sy + SY_MB1 + (((b * 8 + cty) * 4 + (tid & 3)) * 4 + ns) * 256 + slot * 8, tag);
  }
  DG(5);
  acquire_dev();
  u32x4 Wp[NPW][5], Ax[5];
  f32x4 v[2][5];
  {
    const __amdgpu_buffer_rsrc_t rsW = mk_rsrc(ws + OFF_WT1 + (size_t)(ns * 4 + w) * WQ_B, WQ_B);
#pragma unroll
    for (int n = 0; n < 5; ++n)
      Ax[n] = __builtin_amdgcn_raw_buffer_load_b128(rsS, ((w * 20 + ns * 5 + n) * 32 + b * 16 + kg * 4) * 4, (unsigned)OFF_EMB, 0);
#pragma unroll
    for (int k = 0; k < NPW * 5; ++k) Wp[k / 5][k % 5] = __builtin_amdgcn_raw_buffer_load_b128(rsW, G.wvo + k * 1024, 0, 0);
    const int prp = 4 * ty - 1 + lane;
    const bool on = lane < 6 && (unsigned)prp < 32u;
    const char* fp = sy + SY_MB1 + (((b * 8 + ty) * 4 + tx) * 4 + w) * 256 + (lane & 15) * 8;
    wait_words(fp, on, tag, ld_dev(fp));
    acquire_dev();
#pragma unroll
    for (int k = 0; k < NPW; ++k) wait_vm5<-1>(Wp[k]);
    wait_vm5<-1>(Ax);
    DG(6);
    conv_main<1>(G, mk_rsrc(ws + OFF_ACT1, ACT_B), (unsigned)((b * 20 + w * 5) * PLANE_B), rsW, rsW, Wp, Ax, v);
  }
  DG(7);

  const __amdgpu_buffer_rsrc_t rsW2 = mk_rsrc(ws + OFF_WT2 + (size_t)(ns * 4 + w) * WQ_B, WQ_B);
  {
    float s = 0.f;
#pragma unroll
    for (int n = 0; n < 5; ++n)
#pragma unroll
      for (int e = 0; e < 2; ++e) s += (v[e][n][0] + v[e][n][1]) + (v[e][n][2] + v[e][n][3]);
    const float mean = wave_sum(s) * (1.0f / 2560.0f);
    float m2 = 0.f;
#pragma unroll
    for (int n = 0; n < 5; ++n)
#pragma unroll
      for (int e = 0; e < 2; ++e)
#pragma unroll
        for (int j = 0; j < 4; ++j) { float d = v[e][n][j] - mean; m2 += d * d; }
    m2 = wave_sum(m2);
    if (lane == 0) *(LDS_AS f32x2*)(size_t)(wreg + RED_OFF) = (f32x2){mean, m2};
  }
  wg_barrier();
  if (w == 0) publish_stats(ldsb, sy + SY_ST2 + (b * 4 + ns) * 16384 + (ty * 4 + tx) * 16, tag, lane);
  DG(8);
#pragma unroll
  for (int n = 0; n < 5; ++n) {
    gv[n] = *(const u32x2*)(g2 + ns * 80 + n * 16 + kg * 4);
    bv[n] = *(const u32x2*)(be2 + ns * 80 + n * 16 + kg * 4);
  }
#pragma unroll
  for (int k = 0; k < NPW * 5; ++k) Wp[k / 5][k % 5] = __builtin_amdgcn_raw_buffer_load_b128(rsW2, G.wvo + k * 1024, 0, 0);
  if (w == 0) gather_stats(ldsb, sy + SY_ST2 + (b * 4 + ns) * 16384 + (ty * 4 + tx) * 512, tag, lane);
  if (bid == 0 && w == 1) {
    const unsigned t32 = (lane & 1) ? (unsigned)(tag >> 32) : (unsigned)tag;
#pragma unroll 1
    for (int it = 0; it < SPIN_MAX; ++it) {
      bool miss = false;
#pragma unroll
      for (int k = 0; k < 8; ++k) miss |= (unsigned)(ld_dev(sy + SY_ST1 + k * 16384 + lane * 8) >> 32) != t32;
      if (__builtin_amdgcn_ballot_w64(miss) == 0) break;
      __builtin_amdgcn_s_sleep(8);
    }
    if (lane == 0) st_dev(sy + SY_EPOCH, tag - 0x9E3779B97F4A7C15ull + 0x100000001ull);
  }
  wg_barrier();
  DG(9);
  {
    const f32x2 st = *(const LDS_AS f32x2*)(size_t)(ldsb + RED_OFF + 8);
#pragma unroll
    for (int n = 0; n < 5; ++n) {
      unsigned o[2][2];
#pragma unroll
      for (int h = 0; h < 2; ++h) {
        const float a0 = st.y * bf_lo(gv[n][h]), a1 = st.y * bf_hi(gv[n][h]);
        const float b0 = bf_lo(bv[n][h]) - st.x * a0, b1 = bf_hi(bv[n][h]) - st.x * a1;
#pragma unroll
        for (int e = 0; e < 2; ++e) o[e][h] = pk_bf16(silu_f(v[e][n][2 * h] * a0 + b0), silu_f(v[e][n][2 * h + 1] * a1 + b1));
      }
      u32x2 s0 = __builtin_amdgcn_permlane16_swap(o[0][0], o[1][0], false, false);
      u32x2 s1 = __builtin_amdgcn_permlane16_swap(o[0][1], o[1][1], false, false);
      st16_act((u32x4){s0.x, s1.x, s0.y, s1.y}, rsS, dst2, n * PLANE_B);
    }
  }
  DG(10);
  release_dev();
  wg_barrier();
  {
    const int nty = ty - 1 + lane / 3, ntx = tx - 1 + lane % 3;
    const bool on = lane < 9 && (unsigned)nty < 8u && (unsigned)ntx < 4u;
    if (w == 0 && on) st_dev(sy + SY_MB2 + (((b * 8 + nty) * 4 + ntx) * 4 + ns) * 256 + (8 - lane) * 8, tag);
    DG(11);
    const char* fp = sy + SY_MB2 + (((b * 8 + ty) * 4 + tx) * 4 + w) * 256 + (lane & 15) * 8;
    wait_words(fp, on, tag, ld_dev(fp));
    acquire_dev();
#pragma unroll
    for (int k = 0; k < NPW; ++k) wait_vm5<-1>(Wp[k]);
    DG(12);
    conv_main<2>(G, mk_rsrc(ws + OFF_ACT2, ACT_B), (unsigned)((b * 20 + w * 5) * PLANE_B), rsW2, mk_rsrc(x, ACT_B), Wp, Ax, v);
  }
  DG(13);
  {
    char* dst = outp + G.avo;
#pragma unroll
    for (int n = 0; n < 5; ++n) {
      float lo[4], hi[4];
#pragma unroll
      for (int j = 0; j < 4; ++j) {
        u32x2 t = __builtin_amdgcn_permlane16_swap(__float_as_uint(v[0][n][j]), __float_as_uint(v[1][n][j]), false, false);
        lo[j] = __uint_as_float(t.x); hi[j] = __uint_as_float(t.y);
      }
      u32x4 o;
      o.x = pk_bf16(lo[0] + bf_lo(Ax[n].x), lo[1] + bf_hi(Ax[n].x));
      o.y = pk_bf16(lo[2] + bf_lo(Ax[n].y), lo[3] + bf_hi(Ax[n].y));
      o.z = pk_bf16(hi[0] + bf_lo(Ax[n].z), hi[1] + bf_hi(Ax[n].z));
      o.w = pk_bf16(hi[2] + bf_lo(Ax[n].w), hi[3] + bf_hi(Ax[n].w));
      *(u32x4*)(dst + (size_t)n * (16 * NPX * 2)) = o;
    }
  }
}

extern "C" void kernel_launch(void* const* d_in, const int* in_sizes, int n_in, void* d_out, int out_size, void* d_ws,
                              size_t ws_size, hipStream_t stream) {
  resblock_k<<<256, 256, 0, stream>>>((const uint16_t*)d_in[0], (const uint16_t*)d_in[1], (const uint16_t*)d_in[2],
                                      (const uint16_t*)d_in[3], (const uint16_t*)d_in[4], (const uint16_t*)d_in[5],
                                      (const uint16_t*)d_in[6], (const uint16_t*)d_in[7], (const uint16_t*)d_in[8],
                                      (char*)d_out, (char*)d_ws);
}
```

```cpp
#include <hip/hip_runtime.h>
#include <hip/hip_bf16.h>
#include <stdint.h>
#include <utility>

typedef __attribute__((ext_vector_type(8))) __bf16 bf16x8;
typedef __attribute__((ext_vector_type(2))) __bf16 bf16x2;
typedef __attribute__((ext_vector_type(4))) float f32x4;
typedef __attribute__((ext_vector_type(2))) float f32x2;
typedef __attribute__((ext_vector_type(4))) unsigned int u32x4;
typedef __attribute__((ext_vector_type(8))) unsigned int u32x8;
typedef __attribute__((ext_vector_type(2))) unsigned int u32x2;
#define LDS_AS __attribute__((address_space(3)))
#define DEVI __device__ __forceinline__

constexpr int NCH = 320, NPX = 4096, NEMB = 1152;
constexpr int PLANE_B = NPX * 32;
constexpr int ACT_B = 2 * 20 * PLANE_B;
constexpr int NSTEP = 23;
constexpr int WQ_B = NSTEP * 5 * 1024;
constexpr int WT_B = 16 * WQ_B;
constexpr size_t OFF_ACT1 = 0;
constexpr size_t OFF_ACT2 = OFF_ACT1 + ACT_B;
constexpr size_t OFF_WT1 = OFF_ACT2 + ACT_B;
constexpr size_t OFF_WT2 = OFF_WT1 + WT_B;
constexpr size_t OFF_EMB = OFF_WT2 + WT_B;
constexpr size_t OFF_SYNC = OFF_EMB + 40960;
constexpr size_t SY_EPOCH = 0;
constexpr size_t SY_MB1 = 256;
constexpr size_t SY_MB2 = SY_MB1 + 65536;
constexpr size_t SY_ST1 = SY_MB2 + 65536;
constexpr size_t SY_ST2 = SY_ST1 + 131072;
constexpr size_t SY_FJ = SY_ST2 + 131072;
constexpr size_t SY_FE = SY_FJ + 2 * 65536;
constexpr size_t SY_END = SY_FE + 16384;
typedef unsigned long long u64;


template <typename F, int... I>
DEVI void sfor_impl(F&& f, std::integer_sequence<int, I...>) { (f(std::integral_constant<int, I>{}), ...); }
template <int N, typename F>
DEVI void sfor(F&& f) { sfor_impl(f, std::make_integer_sequence<int, N>{}); }

DEVI float bf_lo(unsigned v) { return __uint_as_float(v << 16); }
DEVI float bf_hi(unsigned v) { return __uint_as_float(v & 0xffff0000u); }
DEVI unsigned pk_bf16(float a, float b) {
  bf16x2 r = __builtin_convertvector((f32x2){a, b}, bf16x2);
  return __builtin_bit_cast(unsigned, r);
}
DEVI float silu_f(float y) {
  float e = __builtin_amdgcn_exp2f(y * -1.44269504088896341f);
  return y * __builtin_amdgcn_rcpf(1.0f + e);
}
template <int CTRL>
DEVI float dpp_add(float v) {
  return v + __builtin_bit_cast(float, __builtin_amdgcn_update_dpp(0, __builtin_bit_cast(int, v), CTRL, 0xf, 0xf, true));
}
DEVI float wave_sum(float v) {
  v = dpp_add<0xB1>(v);
  v = dpp_add<0x4E>(v);
  v = dpp_add<0x141>(v);
  v = dpp_add<0x140>(v);
  int iv = __builtin_bit_cast(int, v);
  float a = __builtin_bit_cast(float, __builtin_amdgcn_readlane(iv, 0));
  float b = __builtin_bit_cast(float, __builtin_amdgcn_readlane(iv, 16));
  float c = __builtin_bit_cast(float, __builtin_amdgcn_readlane(iv, 32));
  float d = __builtin_bit_cast(float, __builtin_amdgcn_readlane(iv, 48));
  return (a + b) + (c + d);
}

#define P_TF 6
#define P_NPW 4
#define P_PRO 6
#define P_LEAD 7
constexpr int TF = P_TF;
constexpr int PD = 4;
constexpr int LEAD = P_LEAD;
constexpr int NPW = P_NPW;
#define P_NPW1 2
#define P_PRO1 16
constexpr int NPW1 = P_NPW1;
constexpr int npw(int mode) { return mode == 1 ? NPW1 : NPW; }
constexpr int pro(int mode) { return mode == 1 ? P_PRO1 : P_PRO; }
#define P_G6 116
#define P_DL1 104
#define P_G5 88
#define P_OPG 1000
constexpr int J_DL1 = P_DL1, J_G5 = P_G5, J_G6 = P_G6, J_G7 = J_G6 + 12, J_GE = NSTEP * 8 - 22;
constexpr int NG = NSTEP * 8;
constexpr int GF = (NSTEP - TF) * 8;
constexpr int NOPM = 208;
constexpr int SLAB_B = 6144, WREG_B = 40960;
constexpr int RED_OFF = 3 * SLAB_B + 5760;

struct Sched {
  int nop;
  int ty[NOPM], oa[NOPM], ob[NOPM];
  int opBeg[NG + 2];
  int gs[NG], gr[NG];
  int wW[NG], wD[NG + PD], tieW[NG];
  int wAux;
  int wOp[NOPM];
};
constexpr int step_slab_last(int s) { return s < 5 ? 0 : (s < 14 ? 2 : 4); }
constexpr Sched make_sched(int mode) {
  Sched S{};
  int fu[NSTEP] = {};
  for (int g = 0; g < NG; ++g) {
    if (g < GF) { S.gs[g] = g / 8; S.gr[g] = g % 8; }
    else { int k = g - GF; S.gr[g] = k / TF; S.gs[g] = NSTEP - TF + k % TF; }
  }
  for (int s = 0; s < NSTEP; ++s) fu[s] = s < NSTEP - TF ? 8 * s : GF + (s - (NSTEP - TF));
  int dl[NOPM] = {};
  int n = 0;
  for (int p = 0; p < 6; ++p) { S.ty[n] = 0; S.oa[n] = 0; S.ob[n] = p; dl[n] = -10; ++n; }
  if (mode == 2)
    for (int k = 0; k < 5; ++k) { S.ty[n] = 2; S.oa[n] = k; S.ob[n] = 0; dl[n] = 8 * NSTEP; ++n; }
  if (mode == 1) {
    for (int k = 0; k < 10; ++k) { S.ty[n] = 4; S.oa[n] = k; S.ob[n] = 0; dl[n] = J_DL1 + 2 * k; ++n; }
    S.ty[n] = 5; S.oa[n] = 0; S.ob[n] = 0; dl[n] = J_G5 + 8 * LEAD; ++n;
    S.ty[n] = 6; S.oa[n] = 0; S.ob[n] = 0; dl[n] = J_G6 + 8 * LEAD; ++n;
    for (int k = 0; k < 5; ++k) { S.ty[n] = 2; S.oa[n] = k; S.ob[n] = 0; dl[n] = 1001; ++n; }
    for (int k = 0; k < NPW * 5; ++k) { S.ty[n] = 7; S.oa[n] = k / 5; S.ob[n] = k % 5; dl[n] = 1002; ++n; }
  }
  for (int s = npw(mode); s < NSTEP; ++s)
    for (int k = 0; k < 5; ++k) { S.ty[n] = 1; S.oa[n] = s; S.ob[n] = k; dl[n] = 8 * s; ++n; }
  for (int sl = 1; sl < 5; ++sl)
    for (int p = 0; p < 6; ++p) { S.ty[n] = 0; S.oa[n] = sl; S.ob[n] = p; dl[n] = (sl < 3 ? fu[5] : fu[14]) - PD - 12; ++n; }
  const int NOP = n;
  S.nop = n;
  for (int i = 1; i < NOP; ++i) {
    int t = S.ty[i], a = S.oa[i], b = S.ob[i], d = dl[i], j = i - 1;
    while (j >= 0 && dl[j] > d) { S.ty[j + 1] = S.ty[j]; S.oa[j + 1] = S.oa[j]; S.ob[j + 1] = S.ob[j]; dl[j + 1] = dl[j]; --j; }
    S.ty[j + 1] = t; S.oa[j + 1] = a; S.ob[j + 1] = b; dl[j + 1] = d;
  }
  int lastW[NSTEP] = {}, lastD[5] = {}, lastAux = -1, lastJS = -1;
  for (int s = 0; s < NSTEP; ++s) lastW[s] = -1;
  for (int i = 0; i < NOP; ++i) {
    if (S.ty[i] == 1) lastW[S.oa[i]] = i;
    else if (S.ty[i] == 0) lastD[S.oa[i]] = i;
    else if (S.ty[i] == 2) lastAux = i;
    else if (S.ty[i] == 4) lastJS = i;
    S.wOp[i] = -1;
  }
  int issued = pro(mode), done = -1;
  S.opBeg[0] = 0; S.opBeg[1] = pro(mode);
  auto need = [&](int idx) -> int {
    if (idx <= done) return -1;
    int w = issued - 1 - idx;
    if (w < 0) return -1000;
    if (w > 63) w = 63;
    done = issued - 1 - w;
    return w;
  };
  for (int g = 0; g < PD; ++g) {
    int sl = step_slab_last(S.gs[g]);
    int m = lastD[sl]; if (sl > 0 && lastD[sl - 1] > m) m = lastD[sl - 1];
    S.wD[g] = need(m);
  }
  bool seen[NSTEP] = {};
  int iF = 0;
  for (int g = 0; g < NG; ++g) {
    int s = S.gs[g];
    if (mode == 1 && g == J_GE) S.wAux = need(lastAux);
    S.tieW[g] = (seen[s] || s < npw(mode)) ? 0 : 1;
    S.wW[g] = seen[s] ? -1 : need(lastW[s]);
    seen[s] = true;
    if (g + PD < NG) {
      int sl = step_slab_last(S.gs[g + PD]);
      int md = lastD[sl]; if (sl > 0 && lastD[sl - 1] > md) md = lastD[sl - 1];
      S.wD[g + PD] = need(md);
    }
    for (int rep = 0; rep < (mode == 1 && g >= P_OPG ? 2 : 1) && issued < NOP; ++rep) {
      bool avail = S.ty[issued] != 1 || g >= 8 * (S.oa[issued] - LEAD);
      if (S.ty[issued] == 5) avail = g >= J_G5;
      if (S.ty[issued] == 6) { avail = g >= J_G6; iF = issued; }
      if (mode == 1 && S.ty[issued] == 2) avail = g >= J_G7;
      if (avail && mode == 1 && S.ty[issued] == 2 && S.oa[issued] == 0) S.wOp[issued] = need(iF);
      if (avail && S.ty[issued] == 5) S.wOp[issued] = need(lastJS);
      if (avail) ++issued;
    }
    S.opBeg[g + 2] = issued;
  }
  if (mode == 2) S.wAux = need(lastAux);
  if (issued != NOP) S.wAux = -1000;
  return S;
}
constexpr int step_off(int s) {
  if (s < 3) return s * 32;
  if (s < 5) return (36 + (s - 3)) * 32;
  int q = s - 5, p = q / 9, t = q % 9;
  return (1 + 2 * p) * SLAB_B + ((t / 3) * 18 + t % 3) * 32;
}
constexpr int step_var(int s) { return s < 3 ? 0 : (s < 5 ? 1 : 2); }
constexpr int slot_off(int q) { return q < 18 ? q * 1024 : 5 * SLAB_B + (q - 18) * 1024; }

constexpr int SPIN_MAX = 1 << 15;
template <int N>
DEVI void wait_vm() { asm volatile("s_waitcnt vmcnt(%0)" ::"i"(N) : "memory"); }
template <int N>
DEVI void wait_vm5(u32x4 (&f)[5]) {
  if constexpr (N >= 0)
    asm volatile("s_waitcnt vmcnt(%5)" : "+v"(f[0]), "+v"(f[1]), "+v"(f[2]), "+v"(f[3]), "+v"(f[4]) : "i"(N));
  else
    asm volatile("" : "+v"(f[0]), "+v"(f[1]), "+v"(f[2]), "+v"(f[3]), "+v"(f[4]));
}
DEVI void dma16(unsigned m0v, unsigned vo, __amdgpu_buffer_rsrc_t rs, unsigned so) {
  asm volatile("s_mov_b32 m0, %0\n\ts_nop 0\n\tbuffer_load_dwordx4 %1, %2, %3 offen lds" ::"s"(m0v), "v"(vo), "s"(rs), "s"(so) : "memory");
}
template <int IMM>
DEVI void bload16(u32x4& d, unsigned vo, __amdgpu_buffer_rsrc_t rs, unsigned so) {
  asm volatile("buffer_load_dwordx4 %0, %1, %2, %3 offen offset:%4" : "=v"(d) : "v"(vo), "s"(rs), "s"(so), "i"(IMM));
}
template <int IMM>
DEVI void bload16a(u32x4& d, unsigned vo, __amdgpu_buffer_rsrc_t rs, unsigned so) {
  asm volatile("buffer_load_dwordx4 %0, %1, %2, %3 offen offset:%4" : "=a"(d) : "v"(vo), "s"(rs), "s"(so), "i"(IMM));
}
DEVI void tie5a(u32x4 (&f)[5]) { asm volatile("" : "+a"(f[0]), "+a"(f[1]), "+a"(f[2]), "+a"(f[3]), "+a"(f[4])); }
template <int ST>
DEVI void row_add(float& v) {
  if constexpr (ST == 0) asm volatile("v_add_f32_dpp %0, %0, %0 quad_perm:[1,0,3,2] row_mask:0xf bank_mask:0xf" : "+v"(v));
  else if constexpr (ST == 1) asm volatile("v_add_f32_dpp %0, %0, %0 quad_perm:[2,3,0,1] row_mask:0xf bank_mask:0xf" : "+v"(v));
  else if constexpr (ST == 2) asm volatile("v_add_f32_dpp %0, %0, %0 row_half_mirror row_mask:0xf bank_mask:0xf" : "+v"(v));
  else asm volatile("v_add_f32_dpp %0, %0, %0 row_mirror row_mask:0xf bank_mask:0xf" : "+v"(v));
}
DEVI void bstore16_dev(u32x4 d, unsigned vo, __amdgpu_buffer_rsrc_t rs) {
  asm volatile("buffer_store_dwordx4 %0, %1, %2, 0 offen sc1\n\ts_nop 1" ::"v"(d), "v"(vo), "s"(rs) : "memory");
}
DEVI void bstore8_dev(u64 d, unsigned vo, __amdgpu_buffer_rsrc_t rs) {
  asm volatile("buffer_store_dwordx2 %0, %1, %2, 0 offen sc1" ::"v"(d), "v"(vo), "s"(rs) : "memory");
}
DEVI void bload8_dev(u64& d, unsigned vo, __amdgpu_buffer_rsrc_t rs) {
  asm volatile("buffer_load_dwordx2 %0, %1, %2, 0 offen sc1" : "=v"(d) : "v"(vo), "s"(rs) : "memory");
}
DEVI void spin_words(u64& d, unsigned vo, __amdgpu_buffer_rsrc_t rs, u64 tag, u64 mask) {
  unsigned cnt = SPIN_MAX;
  asm volatile("1:\n\tv_cmp_ne_u64 vcc, %3, %0\n\ts_and_b64 vcc, vcc, %4\n\ts_cbranch_scc0 2f\n\t"
               "s_sub_u32 %1, %1, 1\n\ts_cbranch_scc1 2f\n\ts_sleep 1\n\t"
               "buffer_load_dwordx2 %0, %2, %5, 0 offen sc1\n\ts_waitcnt vmcnt(0)\n\ts_branch 1b\n2:"
               : "+v"(d), "+s"(cnt) : "v"(vo), "s"(tag), "s"(mask), "s"(rs) : "vcc", "scc", "memory");
}
DEVI void wg_barrier() { asm volatile("s_waitcnt lgkmcnt(0)\n\ts_barrier" ::: "memory"); }
DEVI __amdgpu_buffer_rsrc_t mk_rsrc(const void* p, unsigned bytes) {
  return __builtin_amdgcn_make_buffer_rsrc((void*)p, (short)0, bytes, 0x00020000);
}
DEVI void st16_dev(u32x4 v, __amdgpu_buffer_rsrc_t rs, unsigned vo, unsigned so) {
  __builtin_amdgcn_raw_buffer_store_b128(v, rs, vo, so, 16);
}
#define ACT_AUX 16
DEVI void st16_act(u32x4 v, __amdgpu_buffer_rsrc_t rs, unsigned vo, unsigned so) {
  __builtin_amdgcn_raw_buffer_store_b128(v, rs, vo, so, ACT_AUX);
}
DEVI u64 ld_dev(const char* p) { return __hip_atomic_load((const u64*)p, __ATOMIC_RELAXED, __HIP_MEMORY_SCOPE_AGENT); }
DEVI void st_dev(char* p, u64 v) { __hip_atomic_store((u64*)p, v, __ATOMIC_RELAXED, __HIP_MEMORY_SCOPE_AGENT); }
DEVI void wait_words(const char* p, bool on, u64 tag, u64 first) {
  u64 v = first;
#pragma unroll 1
  for (int it = 0; it < SPIN_MAX; ++it) {
    if (__builtin_amdgcn_ballot_w64(on && v != tag) == 0) break;
    __builtin_amdgcn_s_sleep(1);
    v = ld_dev(p);
  }
}
DEVI void acquire_dev() {
  asm volatile("" ::: "memory");
}
DEVI void release_dev() {
  wait_vm<0>();
}

template <int t>
DEVI u32x4 rt_tap(const u32x4 (&jb)[9]) {
  u32x4 ov;
  sfor<4>([&](auto Q) {
    constexpr int q = Q, e0 = 9 * (2 * q) + t, e1 = 9 * (2 * q + 1) + t;
    constexpr unsigned sel = (unsigned)((e0 & 1) * 2) | (unsigned)((e0 & 1) * 2 + 1) << 8 |
                             (unsigned)(4 + (e1 & 1) * 2) << 16 | (unsigned)(4 + (e1 & 1) * 2 + 1) << 24;
    ov[q] = __builtin_amdgcn_perm(jb[e1 >> 3][(e1 >> 1) & 3], jb[e0 >> 3][(e0 >> 1) & 3], sel);
  });
  return ov;
}
struct Job {
  unsigned src, dstS, dstP, flag;
};
template <int t>
DEVI unsigned rt_off(const Job& J) {
  constexpr int dy = t / 3, dx = t % 3;
  constexpr int so = t == 9 ? 3 * 5120 + 512 : (dy < 2 ? dx : (dx == 0 ? 3 : 4)) * 5120 + (dy < 2 ? dy : (dx == 2 ? 1 : 0)) * 512;
  if constexpr (t == 9) return J.dstS + so;
  else return J.dstP ? J.dstP + t * 5120 : J.dstS + so;
}

struct Geo {
  unsigned dvo[6], vb[3][4], wreg, ldsb, wvo, avo;
  int w, lane;
};

template <int MODE>
DEVI void conv_main(const Geo& G_, __amdgpu_buffer_rsrc_t rsA, unsigned dso, __amdgpu_buffer_rsrc_t rsW,
                    __amdgpu_buffer_rsrc_t rsX, u32x4 (&Wp)[NPW][5], u32x4 (&Ax)[5], f32x4 (&v)[2][5],
                    const Job& J, u32x4 (&jb)[9], __amdgpu_buffer_rsrc_t rsJ, __amdgpu_buffer_rsrc_t rsF, unsigned fvo, unsigned evo, u64 tag,
                    __amdgpu_buffer_rsrc_t rsW2
                    ) {
  u64 fj2;
  static constexpr Sched S = make_sched(MODE);
  const unsigned wreg = G_.wreg, ldsb = G_.ldsb;
  const int w = G_.w, lane = G_.lane;
  u32x4 Wf[NSTEP][5];
  u32x4 Pf[PD + 1];
  f32x4 acc[8][5];
  f32x4 T[10], pt[20];
  float E[20];
  sfor<npw(MODE)>([&](auto T) { sfor<5>([&](auto N) { Wf[T][N] = Wp[T][N]; }); });

  auto issue_op = [&](auto K) {
    constexpr int k = K, oa = S.oa[k], ob = S.ob[k];
    if constexpr (S.ty[k] == 0) dma16(wreg + oa * SLAB_B + ob * 1024, G_.dvo[ob], rsA, dso + oa * PLANE_B);
    else if constexpr (S.ty[k] == 1) bload16<((oa * 5 + ob) & 3) * 1024>(Wf[oa][ob], G_.wvo, rsW, ((oa * 5 + ob) >> 2) * 4096);
    else if constexpr (S.ty[k] == 2) {
      if constexpr (MODE == 2) bload16<0>(Ax[oa], G_.avo, rsX, oa * 16 * NPX * 2);
      else {
        if constexpr (oa == 0) {
          if constexpr (S.wOp[k] >= 0) wait_vm<S.wOp[k]>();
          spin_words(fj2, fvo, rsF, tag, 0xFFFFFFFF00007FFFull);
        }
        bload16<oa * 64>(Ax[oa], evo, rsF, 0);
      }
    } else if constexpr (S.ty[k] == 6) bload8_dev(fj2, fvo, rsF);
    else if constexpr (S.ty[k] == 7) bload16a<((oa * 5 + ob) & 3) * 1024>(Wp[oa][ob], G_.wvo, rsW2, ((oa * 5 + ob) >> 2) * 4096);
    else {
      static_assert(S.wOp[k] > -1000, "sched");
      if constexpr (S.wOp[k] >= 0) wait_vm<S.wOp[k]>();
      if constexpr (S.ty[k] == 5) bstore8_dev(tag, J.flag, rsJ);
      else if constexpr (oa == 9) bstore16_dev((u32x4){0u, 0u, 0u, 0u}, rt_off<9>(J), rsJ);
      else {
        if constexpr (oa == 0)
          asm volatile("" : "+v"(jb[0]), "+v"(jb[1]), "+v"(jb[2]), "+v"(jb[3]), "+v"(jb[4]), "+v"(jb[5]), "+v"(jb[6]), "+v"(jb[7]), "+v"(jb[8]));
        bstore16_dev(rt_tap<oa>(jb), rt_off<oa>(J), rsJ);
      }
    }
  };
  auto read_p = [&](auto G) {
    constexpr int g = G, s = S.gs[g], i = S.gr[g];
    if constexpr (S.wD[g] >= 0) wait_vm<S.wD[g]>();
    static_assert(S.wD[g] > -1000, "sched");
    Pf[g % (PD + 1)] = *(const LDS_AS u32x4*)(size_t)(G_.vb[step_var(s)][i >> 1] + step_off(s) + (i & 1) * 576);
  };

  __builtin_amdgcn_sched_barrier(0);
  asm volatile("s_nop 4" ::: "memory");
  sfor<pro(MODE)>([&](auto K) { issue_op(K); });
  sfor<PD>([&](auto G) { read_p(G); });
  __builtin_amdgcn_sched_barrier(0);

  const unsigned wrl = wreg + lane * 16;
  const unsigned wsp = wrl + (w < 2 ? 3456 : 0);
  unsigned rdb[3];
#pragma unroll
  for (int d = 1; d <= 3; ++d) rdb[d - 1] = ldsb + ((w - d) & 3) * WREG_B + lane * 16;
  const unsigned rsp = rdb[2] + (((w - 3) & 3) < 2 ? 3456 : 0);

  sfor<NG>([&](auto G) {
    constexpr int g = G, s = S.gs[g], i = S.gr[g];
    static_assert(S.wW[g] > -1000, "sched");
    if constexpr (S.tieW[g]) wait_vm5<S.wW[g]>(Wf[s]);
    if constexpr (MODE == 1 && g == J_GE) wait_vm5<S.wAux>(Ax);
    constexpr int ft = g >= GF ? (g - GF) % TF : -1;
    if constexpr (g >= GF && ft == 0 && (i == 3 || i == 5 || i == 7))
      asm volatile("s_waitcnt lgkmcnt(1)\n\ts_barrier" ::: "memory");
    const bf16x8 pf = __builtin_bit_cast(bf16x8, Pf[g % (PD + 1)]);
    sfor<5>([&](auto N) {
      constexpr int n = N;
      const bf16x8 wf = __builtin_bit_cast(bf16x8, Wf[s][n]);
      f32x4 cin;
      if constexpr (s == 0) cin = (f32x4){0.f, 0.f, 0.f, 0.f};
      else cin = acc[i][n];
      if constexpr (MODE == 1) acc[i][n] = __builtin_amdgcn_mfma_f32_16x16x32_bf16(wf, pf, cin, 0, 0, 0);
      else acc[i][n] = __builtin_amdgcn_mfma_f32_16x16x32_bf16(pf, wf, cin, 0, 0, 0);
      __builtin_amdgcn_sched_barrier(0);
      if constexpr (MODE == 1 && g >= J_GE && g < J_GE + 16) {
        constexpr int u = (g - J_GE) * 5 + n, st = u / 20, e = u % 20;
        if constexpr (st == 0) E[e] = __uint_as_float(Ax[e / 4][e % 4]);
        row_add<st>(E[e]);
      }
      if constexpr (n == 0 && g + PD < NG) read_p(std::integral_constant<int, (g + PD < NG ? g + PD : 0)>{});
      if constexpr (n == 1)
        sfor<S.opBeg[g + 2] - S.opBeg[g + 1]>([&](auto K) { issue_op(std::integral_constant<int, S.opBeg[g + 1] + K>{}); });
      if constexpr (g >= GF && i >= 1 && i <= 6 && n == 2 && ft < 5) {
        constexpr int q = (i - 1) * 5 + ft;
        if constexpr (q < 28) *(LDS_AS f32x4*)(size_t)(wrl + slot_off(q)) = acc[i - 1][ft];
        else *(LDS_AS f32x4*)(size_t)(wsp + (q - 25) * SLAB_B) = acc[i - 1][ft];
      }
      if constexpr (g >= GF && i >= 3) {
        constexpr int gi = (i - 3) * TF + ft;
        sfor<30>([&](auto Q) {
          constexpr int q = Q;
          constexpr int rg = q < 20 ? (q / 10) * 2 * TF + q % 10 : 4 * TF + (q - 20) / 2;
          constexpr int rn = q < 20 ? 3 : 2 + (q & 1);
          if constexpr (rg == gi && rn == n) {
            f32x4 t;
            if constexpr (q < 28) t = *(const LDS_AS f32x4*)(size_t)(rdb[q / 10] + slot_off(q));
            else t = *(const LDS_AS f32x4*)(size_t)(rsp + (q - 25) * SLAB_B);
            if constexpr (q < 10) T[q] = t; else pt[q - 10] = t;
          }
          if constexpr (q >= 10 && rg + 2 == gi && (q < 20 ? 4 : (q & 1) * 4) == n) T[q % 10] += pt[q - 10];
        });
        if constexpr (i == 7 && ft < 5 && n == 1) {
          if constexpr (MODE == 1) {
            const f32x4 ev = {E[4 * ft], E[4 * ft + 1], E[4 * ft + 2], E[4 * ft + 3]};
            T[ft] += ev; T[5 + ft] += ev;
          }
        }
        if constexpr (i == 7 && ft >= 1 && n == 4) T[ft - 1] += acc[6][ft - 1];
      }
      __builtin_amdgcn_sched_barrier(0);
    });
  });
  static_assert(TF == 6, "final phase");
  T[8] += pt[18]; T[9] += pt[19];
#pragma unroll
  for (int n = 0; n < 5; ++n) { v[0][n] = T[n]; v[1][n] = T[5 + n] + acc[7][n]; }
  static_assert(S.wAux > -1000, "sched");
  if constexpr (MODE == 2) wait_vm5<S.wAux>(Ax);
  else {
    wait_vm<0>();
    sfor<NPW>([&](auto T) { tie5a(Wp[T]); });
  }
}

DEVI void publish_stats(unsigned ldsb, char* ent, u64 tag, int lane) {
  f32x2 e[4];
#pragma unroll
  for (int k = 0; k < 4; ++k) e[k] = *(const LDS_AS f32x2*)(size_t)(ldsb + k * WREG_B + RED_OFF);
  const float mean = ((e[0].x + e[1].x) + (e[2].x + e[3].x)) * 0.25f;
  float m2 = 0.f;
#pragma unroll
  for (int k = 0; k < 4; ++k) { float d = e[k].x - mean; m2 += e[k].y + 2560.0f * d * d; }
  const unsigned t32 = (lane & 1) ? (unsigned)(tag >> 32) : (unsigned)tag;
  st_dev(ent + (lane >> 1) * 512 + (lane & 1) * 8, (u64)__float_as_uint((lane & 1) ? m2 : mean) | (u64)t32 << 32);
}
DEVI void gather_stats(unsigned ldsb, const char* grp, u64 tag, int lane) {
  const unsigned t32 = (lane & 1) ? (unsigned)(tag >> 32) : (unsigned)tag;
  u64 v = ld_dev(grp + lane * 8);
#pragma unroll 1
  for (int it = 0; it < SPIN_MAX; ++it) {
    if (__builtin_amdgcn_ballot_w64((unsigned)(v >> 32) != t32) == 0) break;
    __builtin_amdgcn_s_sleep(1);
    v = ld_dev(grp + lane * 8);
  }
  const float f = __uint_as_float((unsigned)v);
  const float mu = wave_sum((lane & 1) ? 0.f : f) * (1.0f / 32.0f);
  const float d = f - mu;
  const float var = wave_sum((lane & 1) ? f : 10240.0f * d * d) * (1.0f / 327680.0f);
  const float rstd = 1.0f / sqrtf(var + 1e-5f);
  if (lane == 0) *(LDS_AS f32x2*)(size_t)(ldsb + RED_OFF + 8) = (f32x2){mu, rstd};
}

#define FJE 0
#define CODE_B 40960
#define FLQ 0
__global__ __launch_bounds__(256, 1) void resblock_k(const uint16_t* __restrict__ x, char* __restrict__ ws,
                                                     const uint16_t* __restrict__ g1, const uint16_t* __restrict__ be1,
                                                     const uint16_t* __restrict__ w1, const uint16_t* __restrict__ wemb,
                                                     const uint16_t* __restrict__ temb, const uint16_t* __restrict__ w2,
                                                     const uint16_t* __restrict__ g2, const uint16_t* __restrict__ be2,
                                                     char* __restrict__ outp) {
  __shared__ __attribute__((aligned(1024))) char lds[4 * WREG_B];
  const int bid = blockIdx.x, tid = threadIdx.x, lane = tid & 63;
  const int w = __builtin_amdgcn_readfirstlane(tid >> 6);
  const int xcd = bid & 7, idx = bid >> 3;
  const int b = xcd >> 2, ty = 2 * (xcd & 3) + (idx >> 4), tx = (idx >> 2) & 3, ns = idx & 3;
  const int y0 = ty * 8, x0 = tx * 16;
  const int kg = lane >> 4, l15 = lane & 15;
  char* sy = ws + OFF_SYNC;

  const int rp = 8 * (xcd & 3) + (idx >> 2);
  const __amdgpu_buffer_rsrc_t rsX = mk_rsrc(x, ACT_B);
  unsigned xv[20];
#pragma unroll
  for (int i = 0; i < 20; ++i)
    xv[i] = __builtin_amdgcn_raw_buffer_load_b32(rsX, lane * 4, ((b * NCH + ns * 80 + w * 20 + i) * NPX + rp * 128) * 2, 0);
  const unsigned cwarm = __builtin_amdgcn_raw_buffer_load_b32(mk_rsrc((const void*)(__builtin_amdgcn_s_getpc() & ~63ull), CODE_B),
                                                              lane * 8192, (idx * 4 + w) * 64, 0);
  __builtin_amdgcn_sched_barrier(0);
  u64 epoch;
  u32x8 g8, b8;
  u32x2 g2_, b2_;
  asm volatile("s_load_dwordx2 %0, %5, 0\n\ts_load_dwordx8 %1, %6, 0\n\ts_load_dwordx2 %2, %6, 32\n\t"
               "s_load_dwordx8 %3, %7, 0\n\ts_load_dwordx2 %4, %7, 32"
               : "=&s"(epoch), "=&s"(g8), "=&s"(g2_), "=&s"(b8), "=&s"(b2_)
               : "s"((size_t)(sy + SY_EPOCH)), "s"((size_t)(g1 + ns * 80 + w * 20)), "s"((size_t)(be1 + ns * 80 + w * 20)),
                 "s"((size_t)w1), "s"((size_t)wemb), "s"((size_t)temb));
  unsigned ka;
  asm volatile("s_load_dword %0, %1, 64" : "=s"(ka) : "s"((size_t)__builtin_amdgcn_kernarg_segment_ptr()));
  __builtin_amdgcn_sched_barrier(0);
  const int ej = bid - 200 + (w - 1) * 56;
  const bool jw = w == 1 && bid < 200, je = bid >= 200 && (w == 1 || (w == 2 && ej < 80));
  const bool jw2 = w == 2 && bid < 200;
  const int ep = ej / 5, ecg = ej % 5;
  u32x4 jb[9];
  unsigned short tv[9];
  Job J;
  {
    const int ot = bid / 10, jq = bid % 10, oc = jq * 4 + kg;
    const int kq = oc / 10, rr = oc % 10, slab = rr >> 1;
    const unsigned dst = (unsigned)(w == 1 ? OFF_WT1 : OFF_WT2) + ((ot / 5) * 4 + kq) * WQ_B + (ot % 5) * 1024 + l15 * 16 + (rr & 1) * 256;
    J.src = ((ot * 16 + l15) * 2880 + oc * 72) * 2;
    const __amdgpu_buffer_rsrc_t rj = mk_rsrc(je ? wemb : w1, jw ? 320 * 2880 * 2 : (je ? NEMB * NCH * 2 : 0));
    const __amdgpu_buffer_rsrc_t rt_ = mk_rsrc(temb, je ? 2 * NEMB * 2 : 0);
    const unsigned jo = je ? ((ep * 72 + (lane >> 3)) * NCH + ecg * 64 + (lane & 7) * 8) * 2 : J.src, js = je ? 8 * NCH * 2 : 16;
#pragma unroll
    for (int k = 0; k < 9; ++k) tv[k] = __builtin_amdgcn_raw_buffer_load_b16(rt_, ((lane & 1) * NEMB + ep * 72 + (lane >> 3)) * 2, k * 16, 0);
    __builtin_amdgcn_sched_barrier(0);
#pragma unroll
    for (int k = 0; k < 9; ++k) jb[k] = __builtin_amdgcn_raw_buffer_load_b128(rj, jo, k * js, 0);
    __builtin_amdgcn_sched_barrier(0);
    J.dstS = slab == 0 ? dst : 0x80000000u;
    J.dstP = slab == 0 ? 0u : dst + (5 + 9 * ((slab - 1) >> 1)) * 5120 + ((slab - 1) & 1) * 512;
    const unsigned fq = jq - ((5 * kg) >> 1);
    J.flag = fq < 3u ? (unsigned)(OFF_SYNC + SY_FJ) + (w == 1 ? 0 : 65536) + (((ot / 5) * 4 + kg) * 16 + l15) * 256 + ((ot % 5) * 3 + fq) * 8
                     : 0x80000000u;
  }
  const __amdgpu_buffer_rsrc_t rsS = mk_rsrc(ws, (unsigned)(OFF_SYNC + SY_END));
  const unsigned dst1 = (unsigned)OFF_ACT1 + (b * 20 + ns * 5) * PLANE_B + (rp * 128 + (w & 2) * 32) * 32 + lane * 16;
  const unsigned dst2 = (unsigned)OFF_ACT2 + (b * 20 + ns * 5) * PLANE_B + ((y0 + 2 * w + (kg & 1)) * 64 + x0 + l15) * 32 + (kg >> 1) * 16;

  Geo G;
  G.w = w; G.lane = lane;
  G.ldsb = (unsigned)(size_t)(LDS_AS char*)lds;
  G.wreg = G.ldsb + w * WREG_B;
  G.wvo = lane * 16;
  G.avo = (unsigned)((((b * NCH + ns * 80 + l15) * 64 + y0 + 2 * w + (kg & 1)) * 64 + x0 + (kg >> 1) * 8) * 2);
#pragma unroll
  for (int j = 0; j < 6; ++j) {
    const int u = 64 * j + lane, p = u >> 1;
    const int row = (p * 3641) >> 16, col = p - row * 18;
    const int gy = y0 - 1 + row, gx = x0 - 1 + col;
    const bool ok = p < 180 && (unsigned)gy < 64u && (unsigned)gx < 64u;
    G.dvo[j] = ok ? (unsigned)((gy * 64 + gx) * 32 + (u & 1) * 16) : 0x80000000u;
  }
#pragma unroll
  for (int k = 0; k < 4; ++k) {
    const unsigned base = G.wreg + l15 * 32 + (kg & 1) * 16 + ((w + 1 + k) & 3) * 1152;
    G.vb[0][k] = base + (kg >> 1) * 576;
    G.vb[1][k] = base + (kg >> 1) * 32;
    G.vb[2][k] = base + (kg >> 1) * SLAB_B;
  }
  const unsigned ldsb = G.ldsb, wreg = G.wreg;

  {
    float s = 0.f;
#pragma unroll
    for (int i = 0; i < 20; ++i) s += bf_lo(xv[i]) + bf_hi(xv[i]);
    const float mean = wave_sum(s) * (1.0f / 2560.0f);
    float m2 = 0.f;
#pragma unroll
    for (int i = 0; i < 20; ++i) {
      float a = bf_lo(xv[i]) - mean, c = bf_hi(xv[i]) - mean;
      m2 += a * a + c * c;
    }
    m2 = wave_sum(m2);
    if (lane == 0) *(LDS_AS f32x4*)(size_t)(wreg + RED_OFF) = (f32x4){mean, m2, 0.f, -1.f};
  }
  asm volatile("s_waitcnt lgkmcnt(0)" : "+s"(epoch), "+s"(g8), "+s"(g2_), "+s"(b8), "+s"(b2_), "+s"(ka));
  unsigned gs[10], bs[10];
#pragma unroll
  for (int i = 0; i < 10; ++i) { gs[i] = i < 8 ? g8[i & 7] : g2_[i & 1]; bs[i] = i < 8 ? b8[i & 7] : b2_[i & 1]; }
  const u64 tag = epoch + 0x9E3779B97F4A7C15ull;
  wg_barrier();
  if (w == 0) {
    publish_stats(ldsb, sy + SY_ST1 + (b * 4 + ns) * 16384 + rp * 16, tag, lane);
    gather_stats(ldsb, sy + SY_ST1 + (b * 4 + ns) * 16384 + rp * 512, tag, lane);
  } else if (jw) {
    sfor<9>([&](auto T) { st16_dev(rt_tap<T>(jb), rsS, rt_off<T>(J), 0); });
    st16_dev((u32x4){0u, 0u, 0u, 0u}, rsS, rt_off<9>(J), 0);
  } else if (je) {
    float acc[2][8];
#pragma unroll
    for (int i = 0; i < 8; ++i) acc[0][i] = acc[1][i] = 0.f;
#pragma unroll
    for (int k = 0; k < 9; ++k) {
      const float so = silu_f(bf_lo(tv[k]));
      const float sx = __builtin_bit_cast(float, __builtin_amdgcn_update_dpp(0, __builtin_bit_cast(int, so), 0xB1, 0xf, 0xf, true));
      const float s0 = (lane & 1) ? sx : so, s1 = (lane & 1) ? so : sx;
#pragma unroll
      for (int q = 0; q < 4; ++q) {
        const float a = bf_lo(jb[k][q]), c = bf_hi(jb[k][q]);
        acc[0][2 * q] += s0 * a; acc[0][2 * q + 1] += s0 * c;
        acc[1][2 * q] += s1 * a; acc[1][2 * q + 1] += s1 * c;
      }
    }
    float c8[8];
#pragma unroll
    for (int i = 0; i < 8; ++i) {
      u32x2 t = __builtin_amdgcn_permlane16_swap(__float_as_uint(acc[0][i]), __float_as_uint(acc[1][i]), false, false);
      c8[i] = __uint_as_float(t.x) + __uint_as_float(t.y);
    }
    u32x4 o;
#pragma unroll
    for (int i = 0; i < 4; ++i) {
      u32x2 t = __builtin_amdgcn_permlane32_swap(__float_as_uint(c8[i]), __float_as_uint(c8[i + 4]), false, false);
      float d = __uint_as_float(t.x) + __uint_as_float(t.y);
      d = dpp_add<0x128>(d);
      o[i] = __float_as_uint(d);
    }
    if (l15 < 8) st16_dev(o, rsS, (unsigned)OFF_EMB + ((ep * 2 + (kg & 1)) * NCH + ecg * 64 + l15 * 8 + (kg >> 1) * 4) * 4, 0);
    J.flag = lane < 32 && (unsigned)(ecg - kg) < 4u ? (unsigned)(OFF_SYNC + SY_FE) + ((ecg - kg) * 16 + l15) * 256 + (kg * 16 + ep) * 8
                                                    : 0x80000000u;
  }
  asm volatile("" ::"v"(cwarm));
  wg_barrier();

  const unsigned fjo = (unsigned)SY_FJ + ((ns * 4 + w) * 16 + b * 8 + ty) * 256 + l15 * 8;
  u32x4 Wp[NPW][5], Ax[5];
  f32x4 v[2][5];
  if (jw2) {
#pragma unroll
      for (int k = 0; k < 9; ++k) jb[k] = __builtin_amdgcn_raw_buffer_load_b128(mk_rsrc(w2, 320 * 2880 * 2), J.src + k * 16, 0, 0);
    }
  const u64 fj1 = FJE ? ld_dev(sy + fjo) : 0;
  u32x2 gv[5], bv[5];
  const __amdgpu_buffer_rsrc_t rsW = mk_rsrc(ws + OFF_WT1 + (size_t)(ns * 4 + w) * WQ_B, WQ_B);
  {
    const f32x2 st = *(const LDS_AS f32x2*)(size_t)(ldsb + RED_OFF + 8);
#define PREF(k_) do { constexpr int k = k_; Wp[k / 5][k % 5] = __builtin_amdgcn_raw_buffer_load_b128(rsW, G.wvo + k * 1024, 0, 0); } while (0)
    const unsigned stg = ldsb + WREG_B;
    sfor<5>([&](auto Q) {
      constexpr int q = Q;
      unsigned o[2][2];
#pragma unroll
      for (int h = 0; h < 2; ++h) {
        const unsigned gg = gs[2 * q + h], bb = bs[2 * q + h];
        const float a0 = st.y * bf_lo(gg), a1 = st.y * bf_hi(gg);
        const float b0 = bf_lo(bb) - st.x * a0, b1 = bf_hi(bb) - st.x * a1;
        const unsigned u0 = xv[4 * q + 2 * h], u1 = xv[4 * q + 2 * h + 1];
        o[0][h] = pk_bf16(silu_f(bf_lo(u0) * a0 + b0), silu_f(bf_lo(u1) * a1 + b1));
        o[1][h] = pk_bf16(silu_f(bf_hi(u0) * a0 + b0), silu_f(bf_hi(u1) * a1 + b1));
      }
      const int c = w * 20 + q * 4;
      const unsigned ad = stg + (c >> 4) * 4096 + lane * 64 + (c & 4) * 2;
#pragma unroll
      for (int j = 0; j < 2; ++j)
        *(LDS_AS u32x2*)(size_t)(ad + (((j * 2 + ((c >> 3) & 1)) ^ ((lane >> 2) & 3)) << 4)) = (u32x2){o[j][0], o[j][1]};
      __builtin_amdgcn_sched_barrier(0);
      if constexpr (q == FLQ) {
        if (jw || je) { release_dev(); bstore8_dev(tag, J.flag, rsS); }
        __builtin_amdgcn_sched_barrier(0);
      }
    });
    wg_barrier();
    if (w == 0 || w == 3) {
#pragma unroll
      for (int m = 0; m < 10; ++m) {
        const u32x4 q = *(const LDS_AS u32x4*)(size_t)(stg + (m >> 1) * 4096 + ((w & 2) + (m & 1)) * 1024 + ((lane & ~3) << 4) + (((lane & 3) ^ kg) << 4));
        st16_act(q, rsS, dst1 + (m & 1) * 1024, (m >> 1) * PLANE_B);
      }
      release_dev();
    }
  }
  wg_barrier();
  if (tid < 8) {
    const int r3 = rp & 3, up = tid >> 2;
    const int cty = (rp >> 2) + (up ? (r3 == 0 ? -1 : 1) : 0), slot = up ? (r3 == 0 ? 5 : 0) : r3 + 1;
    if (!up || ((r3 == 0 || r3 == 3) && (unsigned)cty < 8u))
      st_dev(sy + SY_MB1 + (((b * 8 + cty) * 4 + (tid & 3)) * 4 + ns) * 256 + slot * 8, tag);
  }
  wait_words(sy + fjo, l15 < 15, tag, fj1);
  sfor<NPW1 * 5>([&](auto K) { PREF(K); });
  const __amdgpu_buffer_rsrc_t rsW2 = mk_rsrc(ws + OFF_WT2 + (size_t)(ns * 4 + w) * WQ_B, WQ_B);
  {
    const int prp = 4 * ty - 1 + lane;
    const bool on = lane < 6 && (unsigned)prp < 32u;
    const char* fp = sy + SY_MB1 + (((b * 8 + ty) * 4 + tx) * 4 + w) * 256 + (lane & 15) * 8;
    wait_words(fp, on, tag, ld_dev(fp));
    acquire_dev();
#pragma unroll
    for (int k = 0; k < NPW1; ++k) wait_vm5<-1>(Wp[k]);
    asm volatile("" : "+v"(jb[0]), "+v"(jb[1]), "+v"(jb[2]), "+v"(jb[3]), "+v"(jb[4]), "+v"(jb[5]), "+v"(jb[6]), "+v"(jb[7]), "+v"(jb[8]));
    conv_main<1>(G, mk_rsrc(ws + OFF_ACT1, ACT_B), (unsigned)((b * 20 + w * 5) * PLANE_B), rsW,
                 rsX, Wp, Ax, v, J, jb, mk_rsrc(ws, jw2 ? (unsigned)(OFF_SYNC + SY_END) : 0u), rsS,
                 (unsigned)OFF_SYNC + (lane < 32 ? fjo + 65536 : (unsigned)SY_FE + (ns * 16 + b * 8 + ty) * 256 + (lane & 31) * 8),
                 (unsigned)OFF_EMB + ((l15 * 2 + b) * NCH + ns * 80 + kg * 4) * 4, tag, rsW2);
  }

  {
    float s = 0.f;
#pragma unroll
    for (int n = 0; n < 5; ++n)
#pragma unroll
      for (int e = 0; e < 2; ++e) s += (v[e][n][0] + v[e][n][1]) + (v[e][n][2] + v[e][n][3]);
    const float mean = wave_sum(s) * (1.0f / 2560.0f);
    float m2 = 0.f;
#pragma unroll
    for (int n = 0; n < 5; ++n)
#pragma unroll
      for (int e = 0; e < 2; ++e)
#pragma unroll
        for (int j = 0; j < 4; ++j) { float d = v[e][n][j] - mean; m2 += d * d; }
    m2 = wave_sum(m2);
    if (lane == 0) *(LDS_AS f32x2*)(size_t)(wreg + RED_OFF) = (f32x2){mean, m2};
  }
  wg_barrier();
  if (w == 0) publish_stats(ldsb, sy + SY_ST2 + (b * 4 + ns) * 16384 + (ty * 4 + tx) * 16, tag, lane);
#pragma unroll
  for (int n = 0; n < 5; ++n) {
    gv[n] = *(const u32x2*)(g2 + ns * 80 + n * 16 + kg * 4);
    bv[n] = *(const u32x2*)(be2 + ns * 80 + n * 16 + kg * 4);
  }
  if (w == 0) gather_stats(ldsb, sy + SY_ST2 + (b * 4 + ns) * 16384 + (ty * 4 + tx) * 512, tag, lane);
  if (bid == 0 && w == 1) {
    const unsigned t32 = (lane & 1) ? (unsigned)(tag >> 32) : (unsigned)tag;
#pragma unroll 1
    for (int it = 0; it < SPIN_MAX; ++it) {
      bool miss = false;
#pragma unroll
      for (int k = 0; k < 8; ++k) miss |= (unsigned)(ld_dev(sy + SY_ST1 + k * 16384 + lane * 8) >> 32) != t32;
      if (__builtin_amdgcn_ballot_w64(miss) == 0) break;
      __builtin_amdgcn_s_sleep(8);
    }
    if (lane == 0) st_dev(sy + SY_EPOCH, tag - 0x9E3779B97F4A7C15ull + 0x100000001ull);
  }
  wg_barrier();
  {
    const f32x2 st = *(const LDS_AS f32x2*)(size_t)(ldsb + RED_OFF + 8);
#pragma unroll
    for (int n = 0; n < 5; ++n) {
      unsigned o[2][2];
#pragma unroll
      for (int h = 0; h < 2; ++h) {
        const float a0 = st.y * bf_lo(gv[n][h]), a1 = st.y * bf_hi(gv[n][h]);
        const float b0 = bf_lo(bv[n][h]) - st.x * a0, b1 = bf_hi(bv[n][h]) - st.x * a1;
#pragma unroll
        for (int e = 0; e < 2; ++e) o[e][h] = pk_bf16(silu_f(v[e][n][2 * h] * a0 + b0), silu_f(v[e][n][2 * h + 1] * a1 + b1));
      }
      u32x2 s0 = __builtin_amdgcn_permlane16_swap(o[0][0], o[1][0], false, false);
      u32x2 s1 = __builtin_amdgcn_permlane16_swap(o[0][1], o[1][1], false, false);
      st16_act((u32x4){s0.x, s1.x, s0.y, s1.y}, rsS, dst2, n * PLANE_B);
    }
  }
  release_dev();
  wg_barrier();
  {
    const int nty = ty - 1 + lane / 3, ntx = tx - 1 + lane % 3;
    const bool on = lane < 9 && (unsigned)nty < 8u && (unsigned)ntx < 4u;
    if (w == 0 && on) st_dev(sy + SY_MB2 + (((b * 8 + nty) * 4 + ntx) * 4 + ns) * 256 + (8 - lane) * 8, tag);
    const char* fp = sy + SY_MB2 + (((b * 8 + ty) * 4 + tx) * 4 + w) * 256 + (lane & 15) * 8;
    wait_words(fp, on, tag, ld_dev(fp));
    acquire_dev();
    conv_main<2>(G, mk_rsrc(ws + OFF_ACT2, ACT_B), (unsigned)((b * 20 + w * 5) * PLANE_B), rsW2, rsX, Wp, Ax, v,
                 J, jb, rsS, rsS, 0u, 0u, tag, rsS);
  }
  {
    char* dst = outp + G.avo;
#pragma unroll
    for (int n = 0; n < 5; ++n) {
      float lo[4], hi[4];
#pragma unroll
      for (int j = 0; j < 4; ++j) {
        u32x2 t = __builtin_amdgcn_permlane16_swap(__float_as_uint(v[0][n][j]), __float_as_uint(v[1][n][j]), false, false);
        lo[j] = __uint_as_float(t.x); hi[j] = __uint_as_float(t.y);
      }
      u32x4 o;
      o.x = pk_bf16(lo[0] + bf_lo(Ax[n].x), lo[1] + bf_hi(Ax[n].x));
      o.y = pk_bf16(lo[2] + bf_lo(Ax[n].y), lo[3] + bf_hi(Ax[n].y));
      o.z = pk_bf16(hi[0] + bf_lo(Ax[n].z), hi[1] + bf_hi(Ax[n].z));
      o.w = pk_bf16(hi[2] + bf_lo(Ax[n].w), hi[3] + bf_hi(Ax[n].w));
      *(u32x4*)(dst + (size_t)n * (16 * NPX * 2)) = o;
    }
  }
}

extern "C" void kernel_launch(void* const* d_in, const int* in_sizes, int n_in, void* d_out, int out_size, void* d_ws,
                              size_t ws_size, hipStream_t stream) {
  resblock_k<<<256, 256, 0, stream>>>((const uint16_t*)d_in[0], (char*)d_ws, (const uint16_t*)d_in[2],
                                      (const uint16_t*)d_in[3], (const uint16_t*)d_in[4], (const uint16_t*)d_in[5],
                                      (const uint16_t*)d_in[1], (const uint16_t*)d_in[8], (const uint16_t*)d_in[6],
                                      (const uint16_t*)d_in[7], (char*)d_out);
}
```

```cpp
#include <hip/hip_runtime.h>
#include <hip/hip_bf16.h>
#include <stdint.h>
#include <utility>

typedef __attribute__((ext_vector_type(8))) __bf16 bf16x8;
typedef __attribute__((ext_vector_type(2))) __bf16 bf16x2;
typedef __attribute__((ext_vector_type(4))) float f32x4;
typedef __attribute__((ext_vector_type(2))) float f32x2;
typedef __attribute__((ext_vector_type(4))) unsigned int u32x4;
typedef __attribute__((ext_vector_type(8))) unsigned int u32x8;
typedef __attribute__((ext_vector_type(2))) unsigned int u32x2;
#define LDS_AS __attribute__((address_space(3)))
#define DEVI __device__ __forceinline__

constexpr int NCH = 320, NPX = 4096, NEMB = 1152;
constexpr int PLANE_B = NPX * 32;
constexpr int ACT_B = 2 * 20 * PLANE_B;
constexpr int NSTEP = 23;
constexpr int WQ_B = NSTEP * 5 * 1024;
constexpr int WT_B = 16 * WQ_B;
constexpr size_t OFF_ACT1 = 0;
constexpr size_t OFF_ACT2 = OFF_ACT1 + ACT_B;
constexpr size_t OFF_WT1 = OFF_ACT2 + ACT_B;
constexpr size_t OFF_WT2 = OFF_WT1 + WT_B;
constexpr size_t OFF_EMB = OFF_WT2 + WT_B;
constexpr size_t OFF_SYNC = OFF_EMB + 40960;
constexpr size_t SY_EPOCH = 0;
constexpr size_t SY_MB1 = 256;
constexpr size_t SY_MB2 = SY_MB1 + 65536;
constexpr size_t SY_ST1 = SY_MB2 + 65536;
constexpr size_t SY_ST2 = SY_ST1 + 131072;
constexpr size_t SY_FJ = SY_ST2 + 131072;
constexpr size_t SY_FE = SY_FJ + 2 * 65536;
constexpr size_t SY_END = SY_FE + 16384;
typedef unsigned long long u64;


template <typename F, int... I>
DEVI void sfor_impl(F&& f, std::integer_sequence<int, I...>) { (f(std::integral_constant<int, I>{}), ...); }
template <int N, typename F>
DEVI void sfor(F&& f) { sfor_impl(f, std::make_integer_sequence<int, N>{}); }

DEVI float bf_lo(unsigned v) { return __uint_as_float(v << 16); }
DEVI float bf_hi(unsigned v) { return __uint_as_float(v & 0xffff0000u); }
DEVI unsigned pk_bf16(float a, float b) {
  bf16x2 r = __builtin_convertvector((f32x2){a, b}, bf16x2);
  return __builtin_bit_cast(unsigned, r);
}
DEVI float silu_f(float y) {
  float e = __builtin_amdgcn_exp2f(y * -1.44269504088896341f);
  return y * __builtin_amdgcn_rcpf(1.0f + e);
}
template <int CTRL>
DEVI float dpp_add(float v) {
  return v + __builtin_bit_cast(float, __builtin_amdgcn_update_dpp(0, __builtin_bit_cast(int, v), CTRL, 0xf, 0xf, true));
}
DEVI float wave_sum(float v) {
  v = dpp_add<0xB1>(v);
  v = dpp_add<0x4E>(v);
  v = dpp_add<0x141>(v);
  v = dpp_add<0x140>(v);
  int iv = __builtin_bit_cast(int, v);
  float a = __builtin_bit_cast(float, __builtin_amdgcn_readlane(iv, 0));
  float b = __builtin_bit_cast(float, __builtin_amdgcn_readlane(iv, 16));
  float c = __builtin_bit_cast(float, __builtin_amdgcn_readlane(iv, 32));
  float d = __builtin_bit_cast(float, __builtin_amdgcn_readlane(iv, 48));
  return (a + b) + (c + d);
}

#define P_TF 6
#define P_NPW 4
#define P_PRO 6
#define P_LEAD 7
constexpr int TF = P_TF;
constexpr int PD = 4;
constexpr int LEAD = P_LEAD;
constexpr int NPW = P_NPW;
#define P_NPW1 2
#define P_PRO1 11
constexpr int NPW1 = P_NPW1;
constexpr int npw(int mode) { return mode == 1 ? NPW1 : NPW; }
constexpr int pro(int mode) { return mode == 1 ? P_PRO1 : P_PRO; }
#define P_G6 116
#define P_DL1 104
#define P_G5 88
#define P_GE 162
#define P_DD 12
#define P_OPG 1000
constexpr int J_DL1 = P_DL1, J_G5 = P_G5, J_G6 = P_G6, J_G7 = J_G6 + 12, J_GE = P_GE;
constexpr int NG = NSTEP * 8;
constexpr int GF = (NSTEP - TF) * 8;
constexpr int NOPM = 208;
constexpr int SLAB_B = 6144, WREG_B = 40960;
constexpr int RED_OFF = 3 * SLAB_B + 5760;

struct Sched {
  int nop;
  int ty[NOPM], oa[NOPM], ob[NOPM];
  int opBeg[NG + 2];
  int gs[NG], gr[NG];
  int wW[NG], wD[NG + PD], tieW[NG];
  int wAux;
  int wOp[NOPM];
};
constexpr int step_slab_last(int s) { return s < 5 ? 0 : (s < 14 ? 2 : 4); }
constexpr Sched make_sched(int mode) {
  Sched S{};
  int fu[NSTEP] = {};
  for (int g = 0; g < NG; ++g) {
    if (g < GF) { S.gs[g] = g / 8; S.gr[g] = g % 8; }
    else { int k = g - GF; S.gr[g] = k / TF; S.gs[g] = NSTEP - TF + k % TF; }
  }
  for (int s = 0; s < NSTEP; ++s) fu[s] = s < NSTEP - TF ? 8 * s : GF + (s - (NSTEP - TF));
  int dl[NOPM] = {};
  int n = 0;
  for (int p = 0; p < 6; ++p) { S.ty[n] = 0; S.oa[n] = 0; S.ob[n] = p; dl[n] = -10; ++n; }
  if (mode == 2)
    for (int k = 0; k < 5; ++k) { S.ty[n] = 2; S.oa[n] = k; S.ob[n] = 0; dl[n] = 8 * NSTEP; ++n; }
  if (mode == 1) {
    for (int k = 0; k < 10; ++k) { S.ty[n] = 4; S.oa[n] = k; S.ob[n] = 0; dl[n] = J_DL1 + 2 * k; ++n; }
    S.ty[n] = 5; S.oa[n] = 0; S.ob[n] = 0; dl[n] = J_G5 + 8 * LEAD; ++n;
    S.ty[n] = 6; S.oa[n] = 0; S.ob[n] = 0; dl[n] = J_G6 + 8 * LEAD; ++n;
    for (int k = 0; k < 5; ++k) { S.ty[n] = 2; S.oa[n] = k; S.ob[n] = 0; dl[n] = 1001; ++n; }
    for (int k = 0; k < NPW * 5; ++k) { S.ty[n] = 7; S.oa[n] = k / 5; S.ob[n] = k % 5; dl[n] = 1002; ++n; }
  }
  for (int s = npw(mode); s < NSTEP; ++s)
    for (int k = 0; k < 5; ++k) { S.ty[n] = 1; S.oa[n] = s; S.ob[n] = k; dl[n] = 8 * s; ++n; }
  for (int sl = 1; sl < 5; ++sl)
    for (int p = 0; p < 6; ++p) { S.ty[n] = 0; S.oa[n] = sl; S.ob[n] = p; dl[n] = (sl < 3 ? fu[5] : fu[14]) - PD - (mode == 1 && sl < 3 ? P_DD : 12); ++n; }
  const int NOP = n;
  S.nop = n;
  for (int i = 1; i < NOP; ++i) {
    int t = S.ty[i], a = S.oa[i], b = S.ob[i], d = dl[i], j = i - 1;
    while (j >= 0 && dl[j] > d) { S.ty[j + 1] = S.ty[j]; S.oa[j + 1] = S.oa[j]; S.ob[j + 1] = S.ob[j]; dl[j + 1] = dl[j]; --j; }
    S.ty[j + 1] = t; S.oa[j + 1] = a; S.ob[j + 1] = b; dl[j + 1] = d;
  }
  int lastW[NSTEP] = {}, lastD[5] = {}, lastAux = -1, lastJS = -1;
  for (int s = 0; s < NSTEP; ++s) lastW[s] = -1;
  for (int i = 0; i < NOP; ++i) {
    if (S.ty[i] == 1) lastW[S.oa[i]] = i;
    else if (S.ty[i] == 0) lastD[S.oa[i]] = i;
    else if (S.ty[i] == 2) lastAux = i;
    else if (S.ty[i] == 4) lastJS = i;
    S.wOp[i] = -1;
  }
  int issued = pro(mode), done = -1;
  S.opBeg[0] = 0; S.opBeg[1] = pro(mode);
  auto need = [&](int idx) -> int {
    if (idx <= done) return -1;
    int w = issued - 1 - idx;
    if (w < 0) return -1000;
    if (w > 63) w = 63;
    done = issued - 1 - w;
    return w;
  };
  for (int g = 0; g < PD; ++g) {
    int sl = step_slab_last(S.gs[g]);
    int m = lastD[sl]; if (sl > 0 && lastD[sl - 1] > m) m = lastD[sl - 1];
    S.wD[g] = need(m);
  }
  bool seen[NSTEP] = {};
  int iF = 0;
  for (int g = 0; g < NG; ++g) {
    int s = S.gs[g];
    if (mode == 1 && g == J_GE) S.wAux = need(lastAux);
    S.tieW[g] = (seen[s] || s < npw(mode)) ? 0 : 1;
    S.wW[g] = seen[s] ? -1 : need(lastW[s]);
    seen[s] = true;
    if (g + PD < NG) {
      int sl = step_slab_last(S.gs[g + PD]);
      int md = lastD[sl]; if (sl > 0 && lastD[sl - 1] > md) md = lastD[sl - 1];
      S.wD[g + PD] = need(md);
    }
    for (int rep = 0; rep < (mode == 1 && g >= P_OPG ? 2 : 1) && issued < NOP; ++rep) {
      bool avail = S.ty[issued] != 1 || g >= 8 * (S.oa[issued] - LEAD);
      if (S.ty[issued] == 5) avail = g >= J_G5;
      if (S.ty[issued] == 6) { avail = g >= J_G6; iF = issued; }
      if (mode == 1 && S.ty[issued] == 2) avail = g >= J_G7;
      if (avail && mode == 1 && S.ty[issued] == 2 && S.oa[issued] == 0) S.wOp[issued] = need(iF);
      if (avail && S.ty[issued] == 5) S.wOp[issued] = need(lastJS);
      if (avail) ++issued;
    }
    S.opBeg[g + 2] = issued;
  }
  if (mode == 2) S.wAux = need(lastAux);
  if (issued != NOP) S.wAux = -1000;
  return S;
}
constexpr int step_off(int s) {
  if (s < 3) return s * 32;
  if (s < 5) return (36 + (s - 3)) * 32;
  int q = s - 5, p = q / 9, t = q % 9;
  return (1 + 2 * p) * SLAB_B + ((t / 3) * 18 + t % 3) * 32;
}
constexpr int step_var(int s) { return s < 3 ? 0 : (s < 5 ? 1 : 2); }
constexpr int slot_off(int q) { return q < 18 ? q * 1024 : 5 * SLAB_B + (q - 18) * 1024; }

constexpr int SPIN_MAX = 1 << 15;
template <int N>
DEVI void wait_vm() { asm volatile("s_waitcnt vmcnt(%0)" ::"i"(N) : "memory"); }
template <int N>
DEVI void wait_vm5(u32x4 (&f)[5]) {
  if constexpr (N >= 0)
    asm volatile("s_waitcnt vmcnt(%5)" : "+v"(f[0]), "+v"(f[1]), "+v"(f[2]), "+v"(f[3]), "+v"(f[4]) : "i"(N));
  else
    asm volatile("" : "+v"(f[0]), "+v"(f[1]), "+v"(f[2]), "+v"(f[3]), "+v"(f[4]));
}
DEVI void dma16(unsigned m0v, unsigned vo, __amdgpu_buffer_rsrc_t rs, unsigned so) {
  asm volatile("s_mov_b32 m0, %0\n\ts_nop 0\n\tbuffer_load_dwordx4 %1, %2, %3 offen lds" ::"s"(m0v), "v"(vo), "s"(rs), "s"(so) : "memory");
}
template <int IMM>
DEVI void bload16(u32x4& d, unsigned vo, __amdgpu_buffer_rsrc_t rs, unsigned so) {
  asm volatile("buffer_load_dwordx4 %0, %1, %2, %3 offen offset:%4" : "=v"(d) : "v"(vo), "s"(rs), "s"(so), "i"(IMM));
}
template <int IMM>
DEVI void bload16a(u32x4& d, unsigned vo, __amdgpu_buffer_rsrc_t rs, unsigned so) {
  asm volatile("buffer_load_dwordx4 %0, %1, %2, %3 offen offset:%4" : "=a"(d) : "v"(vo), "s"(rs), "s"(so), "i"(IMM));
}
DEVI void tie5a(u32x4 (&f)[5]) { asm volatile("" : "+a"(f[0]), "+a"(f[1]), "+a"(f[2]), "+a"(f[3]), "+a"(f[4])); }
template <int ST>
DEVI void row_add(float& v) {
  if constexpr (ST == 0) asm volatile("v_add_f32_dpp %0, %0, %0 quad_perm:[1,0,3,2] row_mask:0xf bank_mask:0xf" : "+v"(v));
  else if constexpr (ST == 1) asm volatile("v_add_f32_dpp %0, %0, %0 quad_perm:[2,3,0,1] row_mask:0xf bank_mask:0xf" : "+v"(v));
  else if constexpr (ST == 2) asm volatile("v_add_f32_dpp %0, %0, %0 row_half_mirror row_mask:0xf bank_mask:0xf" : "+v"(v));
  else asm volatile("v_add_f32_dpp %0, %0, %0 row_mirror row_mask:0xf bank_mask:0xf" : "+v"(v));
}
DEVI void bstore16_dev(u32x4 d, unsigned vo, __amdgpu_buffer_rsrc_t rs) {
  asm volatile("buffer_store_dwordx4 %0, %1, %2, 0 offen sc1\n\ts_nop 1" ::"v"(d), "v"(vo), "s"(rs) : "memory");
}
DEVI void bstore8_dev(u64 d, unsigned vo, __amdgpu_buffer_rsrc_t rs) {
  asm volatile("buffer_store_dwordx2 %0, %1, %2, 0 offen sc1" ::"v"(d), "v"(vo), "s"(rs) : "memory");
}
DEVI void bload8_dev(u64& d, unsigned vo, __amdgpu_buffer_rsrc_t rs) {
  asm volatile("buffer_load_dwordx2 %0, %1, %2, 0 offen sc1" : "=v"(d) : "v"(vo), "s"(rs) : "memory");
}
DEVI void spin_words(u64& d, unsigned vo, __amdgpu_buffer_rsrc_t rs, u64 tag, u64 mask) {
  unsigned cnt = SPIN_MAX;
  asm volatile("1:\n\tv_cmp_ne_u64 vcc, %3, %0\n\ts_and_b64 vcc, vcc, %4\n\ts_cbranch_scc0 2f\n\t"
               "s_sub_u32 %1, %1, 1\n\ts_cbranch_scc1 2f\n\ts_sleep 1\n\t"
               "buffer_load_dwordx2 %0, %2, %5, 0 offen sc1\n\ts_waitcnt vmcnt(0)\n\ts_branch 1b\n2:"
               : "+v"(d), "+s"(cnt) : "v"(vo), "s"(tag), "s"(mask), "s"(rs) : "vcc", "scc", "memory");
}
DEVI void wg_barrier() { asm volatile("s_waitcnt lgkmcnt(0)\n\ts_barrier" ::: "memory"); }
DEVI __amdgpu_buffer_rsrc_t mk_rsrc(const void* p, unsigned bytes) {
  return __builtin_amdgcn_make_buffer_rsrc((void*)p, (short)0, bytes, 0x00020000);
}
DEVI void st16_dev(u32x4 v, __amdgpu_buffer_rsrc_t rs, unsigned vo, unsigned so) {
  __builtin_amdgcn_raw_buffer_store_b128(v, rs, vo, so, 16);
}
#define ACT_AUX 16
DEVI void st16_act(u32x4 v, __amdgpu_buffer_rsrc_t rs, unsigned vo, unsigned so) {
  __builtin_amdgcn_raw_buffer_store_b128(v, rs, vo, so, ACT_AUX);
}
DEVI u64 ld_dev(const char* p) { return __hip_atomic_load((const u64*)p, __ATOMIC_RELAXED, __HIP_MEMORY_SCOPE_AGENT); }
DEVI void st_dev(char* p, u64 v) { __hip_atomic_store((u64*)p, v, __ATOMIC_RELAXED, __HIP_MEMORY_SCOPE_AGENT); }
DEVI void wait_words(const char* p, bool on, u64 tag, u64 first) {
  u64 v = first;
#pragma unroll 1
  for (int it = 0; it < SPIN_MAX; ++it) {
    if (__builtin_amdgcn_ballot_w64(on && v != tag) == 0) break;
    __builtin_amdgcn_s_sleep(1);
    v = ld_dev(p);
  }
}
DEVI void acquire_dev() {
  asm volatile("" ::: "memory");
}
DEVI void release_dev() {
  wait_vm<0>();
}

template <int t>
DEVI u32x4 rt_tap(const u32x4 (&jb)[9]) {
  u32x4 ov;
  sfor<4>([&](auto Q) {
    constexpr int q = Q, e0 = 9 * (2 * q) + t, e1 = 9 * (2 * q + 1) + t;
    constexpr unsigned sel = (unsigned)((e0 & 1) * 2) | (unsigned)((e0 & 1) * 2 + 1) << 8 |
                             (unsigned)(4 + (e1 & 1) * 2) << 16 | (unsigned)(4 + (e1 & 1) * 2 + 1) << 24;
    ov[q] = __builtin_amdgcn_perm(jb[e1 >> 3][(e1 >> 1) & 3], jb[e0 >> 3][(e0 >> 1) & 3], sel);
  });
  return ov;
}
struct Job {
  unsigned src, dstS, dstP, flag;
};
template <int t>
DEVI unsigned rt_off(const Job& J) {
  constexpr int dy = t / 3, dx = t % 3;
  constexpr int so = t == 9 ? 3 * 5120 + 512 : (dy < 2 ? dx : (dx == 0 ? 3 : 4)) * 5120 + (dy < 2 ? dy : (dx == 2 ? 1 : 0)) * 512;
  if constexpr (t == 9) return J.dstS + so;
  else return J.dstP ? J.dstP + t * 5120 : J.dstS + so;
}

struct Geo {
  unsigned dvo[6], vb[3][4], wreg, ldsb, wvo, avo;
  int w, lane;
};

template <int MODE>
DEVI void conv_main(const Geo& G_, __amdgpu_buffer_rsrc_t rsA, unsigned dso, __amdgpu_buffer_rsrc_t rsW,
                    __amdgpu_buffer_rsrc_t rsX, u32x4 (&Wp)[NPW][5], u32x4 (&Ax)[5], f32x4 (&v)[2][5],
                    const Job& J, u32x4 (&jb)[9], __amdgpu_buffer_rsrc_t rsJ, __amdgpu_buffer_rsrc_t rsF, unsigned fvo, unsigned evo, u64 tag,
                    __amdgpu_buffer_rsrc_t rsW2
                    ) {
  u64 fj2;
  static constexpr Sched S = make_sched(MODE);
  const unsigned wreg = G_.wreg, ldsb = G_.ldsb;
  const int w = G_.w, lane = G_.lane;
  u32x4 Wf[NSTEP][5];
  u32x4 Pf[PD + 1];
  f32x4 acc[8][5];
  f32x4 T[10], pt[20];
  float E[20];
  sfor<npw(MODE)>([&](auto T) { sfor<5>([&](auto N) { Wf[T][N] = Wp[T][N]; }); });

  auto issue_op = [&](auto K) {
    constexpr int k = K, oa = S.oa[k], ob = S.ob[k];
    if constexpr (S.ty[k] == 0) dma16(wreg + oa * SLAB_B + ob * 1024, G_.dvo[ob], rsA, dso + oa * PLANE_B);
    else if constexpr (S.ty[k] == 1) bload16<((oa * 5 + ob) & 3) * 1024>(Wf[oa][ob], G_.wvo, rsW, ((oa * 5 + ob) >> 2) * 4096);
    else if constexpr (S.ty[k] == 2) {
      if constexpr (MODE == 2) bload16<0>(Ax[oa], G_.avo, rsX, oa * 16 * NPX * 2);
      else {
        if constexpr (oa == 0) {
          if constexpr (S.wOp[k] >= 0) wait_vm<S.wOp[k]>();
          spin_words(fj2, fvo, rsF, tag, 0xFFFFFFFF00007FFFull);
        }
        bload16<oa * 64>(Ax[oa], evo, rsF, 0);
      }
    } else if constexpr (S.ty[k] == 6) bload8_dev(fj2, fvo, rsF);
    else if constexpr (S.ty[k] == 7) bload16a<((oa * 5 + ob) & 3) * 1024>(Wp[oa][ob], G_.wvo, rsW2, ((oa * 5 + ob) >> 2) * 4096);
    else {
      static_assert(S.wOp[k] > -1000, "sched");
      if constexpr (S.wOp[k] >= 0) wait_vm<S.wOp[k]>();
      if constexpr (S.ty[k] == 5) bstore8_dev(tag, J.flag, rsJ);
      else if constexpr (oa == 9) bstore16_dev((u32x4){0u, 0u, 0u, 0u}, rt_off<9>(J), rsJ);
      else {
        if constexpr (oa == 0)
          asm volatile("" : "+v"(jb[0]), "+v"(jb[1]), "+v"(jb[2]), "+v"(jb[3]), "+v"(jb[4]), "+v"(jb[5]), "+v"(jb[6]), "+v"(jb[7]), "+v"(jb[8]));
        bstore16_dev(rt_tap<oa>(jb), rt_off<oa>(J), rsJ);
      }
    }
  };
  auto read_p = [&](auto G) {
    constexpr int g = G, s = S.gs[g], i = S.gr[g];
    if constexpr (S.wD[g] >= 0) wait_vm<S.wD[g]>();
    static_assert(S.wD[g] > -1000, "sched");
    Pf[g % (PD + 1)] = *(const LDS_AS u32x4*)(size_t)(G_.vb[step_var(s)][i >> 1] + step_off(s) + (i & 1) * 576);
  };

  __builtin_amdgcn_sched_barrier(0);
  asm volatile("s_nop 4" ::: "memory");
  sfor<pro(MODE)>([&](auto K) { issue_op(K); });
  sfor<PD>([&](auto G) { read_p(G); });
  __builtin_amdgcn_sched_barrier(0);

  const unsigned wrl = wreg + lane * 16;
  const unsigned wsp = wrl + (w < 2 ? 3456 : 0);
  unsigned rdb[3];
#pragma unroll
  for (int d = 1; d <= 3; ++d) rdb[d - 1] = ldsb + ((w - d) & 3) * WREG_B + lane * 16;
  const unsigned rsp = rdb[2] + (((w - 3) & 3) < 2 ? 3456 : 0);

  sfor<NG>([&](auto G) {
    constexpr int g = G, s = S.gs[g], i = S.gr[g];
    static_assert(S.wW[g] > -1000, "sched");
    if constexpr (S.tieW[g]) wait_vm5<S.wW[g]>(Wf[s]);
    if constexpr (MODE == 1 && g == J_GE) wait_vm5<S.wAux>(Ax);
    constexpr int ft = g >= GF ? (g - GF) % TF : -1;
    if constexpr (g >= GF && ft == 0 && (i == 3 || i == 5 || i == 7))
      asm volatile("s_waitcnt lgkmcnt(1)\n\ts_barrier" ::: "memory");
    const bf16x8 pf = __builtin_bit_cast(bf16x8, Pf[g % (PD + 1)]);
    sfor<5>([&](auto N) {
      constexpr int n = N;
      const bf16x8 wf = __builtin_bit_cast(bf16x8, Wf[s][n]);
      f32x4 cin;
      if constexpr (s == 0) cin = (f32x4){0.f, 0.f, 0.f, 0.f};
      else cin = acc[i][n];
      if constexpr (MODE == 1) acc[i][n] = __builtin_amdgcn_mfma_f32_16x16x32_bf16(wf, pf, cin, 0, 0, 0);
      else acc[i][n] = __builtin_amdgcn_mfma_f32_16x16x32_bf16(pf, wf, cin, 0, 0, 0);
      __builtin_amdgcn_sched_barrier(0);
      if constexpr (MODE == 1 && g >= J_GE && g < J_GE + 16) {
        constexpr int u = (g - J_GE) * 5 + n, st = u / 20, e = u % 20;
        if constexpr (st == 0) E[e] = __uint_as_float(Ax[e / 4][e % 4]);
        row_add<st>(E[e]);
      }
      if constexpr (n == 0 && g + PD < NG) read_p(std::integral_constant<int, (g + PD < NG ? g + PD : 0)>{});
      if constexpr (n == 1)
        sfor<S.opBeg[g + 2] - S.opBeg[g + 1]>([&](auto K) { issue_op(std::integral_constant<int, S.opBeg[g + 1] + K>{}); });
      if constexpr (g >= GF && i >= 1 && i <= 6 && n == 2 && ft < 5) {
        constexpr int q = (i - 1) * 5 + ft;
        if constexpr (q < 28) *(LDS_AS f32x4*)(size_t)(wrl + slot_off(q)) = acc[i - 1][ft];
        else *(LDS_AS f32x4*)(size_t)(wsp + (q - 25) * SLAB_B) = acc[i - 1][ft];
      }
      if constexpr (g >= GF && i >= 3) {
        constexpr int gi = (i - 3) * TF + ft;
        sfor<30>([&](auto Q) {
          constexpr int q = Q;
          constexpr int rg = q < 20 ? (q / 10) * 2 * TF + q % 10 : 4 * TF + (q - 20) / 2;
          constexpr int rn = q < 20 ? 3 : 2 + (q & 1);
          if constexpr (rg == gi && rn == n) {
            f32x4 t;
            if constexpr (q < 28) t = *(const LDS_AS f32x4*)(size_t)(rdb[q / 10] + slot_off(q));
            else t = *(const LDS_AS f32x4*)(size_t)(rsp + (q - 25) * SLAB_B);
            if constexpr (q < 10) T[q] = t; else pt[q - 10] = t;
          }
          if constexpr (q >= 10 && rg + 2 == gi && (q < 20 ? 4 : (q & 1) * 4) == n) T[q % 10] += pt[q - 10];
        });
        if constexpr (i == 7 && ft < 5 && n == 1) {
          if constexpr (MODE == 1) {
            const f32x4 ev = {E[4 * ft], E[4 * ft + 1], E[4 * ft + 2], E[4 * ft + 3]};
            T[ft] += ev; T[5 + ft] += ev;
          }
        }
        if constexpr (i == 7 && ft >= 1 && n == 4) T[ft - 1] += acc[6][ft - 1];
      }
      __builtin_amdgcn_sched_barrier(0);
    });
  });
  static_assert(TF == 6, "final phase");
  T[8] += pt[18]; T[9] += pt[19];
#pragma unroll
  for (int n = 0; n < 5; ++n) { v[0][n] = T[n]; v[1][n] = T[5 + n] + acc[7][n]; }
  static_assert(S.wAux > -1000, "sched");
  if constexpr (MODE == 2) wait_vm5<S.wAux>(Ax);
  else {
    wait_vm<0>();
    sfor<NPW>([&](auto T) { tie5a(Wp[T]); });
  }
}

DEVI void publish_stats(unsigned ldsb, char* ent, u64 tag, int lane) {
  f32x2 e[4];
#pragma unroll
  for (int k = 0; k < 4; ++k) e[k] = *(const LDS_AS f32x2*)(size_t)(ldsb + k * WREG_B + RED_OFF);
  const float mean = ((e[0].x + e[1].x) + (e[2].x + e[3].x)) * 0.25f;
  float m2 = 0.f;
#pragma unroll
  for (int k = 0; k < 4; ++k) { float d = e[k].x - mean; m2 += e[k].y + 2560.0f * d * d; }
  const unsigned t32 = (lane & 1) ? (unsigned)(tag >> 32) : (unsigned)tag;
  st_dev(ent + (lane >> 1) * 512 + (lane & 1) * 8, (u64)__float_as_uint((lane & 1) ? m2 : mean) | (u64)t32 << 32);
}
DEVI void gather_stats(unsigned ldsb, const char* grp, u64 tag, int lane) {
  const unsigned t32 = (lane & 1) ? (unsigned)(tag >> 32) : (unsigned)tag;
  u64 v = ld_dev(grp + lane * 8);
#pragma unroll 1
  for (int it = 0; it < SPIN_MAX; ++it) {
    if (__builtin_amdgcn_ballot_w64((unsigned)(v >> 32) != t32) == 0) break;
    __builtin_amdgcn_s_sleep(1);
    v = ld_dev(grp + lane * 8);
  }
  const float f = __uint_as_float((unsigned)v);
  const float mu = wave_sum((lane & 1) ? 0.f : f) * (1.0f / 32.0f);
  const float d = f - mu;
  const float var = wave_sum((lane & 1) ? f : 10240.0f * d * d) * (1.0f / 327680.0f);
  const float rstd = 1.0f / sqrtf(var + 1e-5f);
  if (lane == 0) *(LDS_AS f32x2*)(size_t)(ldsb + RED_OFF + 8) = (f32x2){mu, rstd};
}

#define FJE 1
#define CODE_B 40960
#define FLQ 0
__global__ __launch_bounds__(256, 1) void resblock_k(const uint16_t* __restrict__ x, char* __restrict__ ws,
                                                     const uint16_t* __restrict__ g1, const uint16_t* __restrict__ be1,
                                                     const uint16_t* __restrict__ w1, const uint16_t* __restrict__ wemb,
                                                     const uint16_t* __restrict__ temb, const uint16_t* __restrict__ w2,
                                                     const uint16_t* __restrict__ g2, const uint16_t* __restrict__ be2,
                                                     char* __restrict__ outp) {
  __shared__ __attribute__((aligned(1024))) char lds[4 * WREG_B];
  const int bid = blockIdx.x, tid = threadIdx.x, lane = tid & 63;
  const int w = __builtin_amdgcn_readfirstlane(tid >> 6);
  const int xcd = bid & 7, idx = bid >> 3;
  const int b = xcd >> 2, ty = 2 * (xcd & 3) + (idx >> 4), tx = (idx >> 2) & 3, ns = idx & 3;
  const int y0 = ty * 8, x0 = tx * 16;
  const int kg = lane >> 4, l15 = lane & 15;
  char* sy = ws + OFF_SYNC;

  const int rp = 8 * (xcd & 3) + (idx >> 2);
  const __amdgpu_buffer_rsrc_t rsX = mk_rsrc(x, ACT_B);
  unsigned xv[20];
#pragma unroll
  for (int i = 0; i < 20; ++i)
    xv[i] = __builtin_amdgcn_raw_buffer_load_b32(rsX, lane * 4, ((b * NCH + ns * 80 + w * 20 + i) * NPX + rp * 128) * 2, 0);
  const unsigned cwarm = __builtin_amdgcn_raw_buffer_load_b32(mk_rsrc((const void*)(__builtin_amdgcn_s_getpc() & ~63ull), CODE_B),
                                                              lane * 8192, (idx * 4 + w) * 64, 0);
  __builtin_amdgcn_sched_barrier(0);
  u64 epoch;
  u32x8 g8, b8;
  u32x2 g2_, b2_;
  asm volatile("s_load_dwordx2 %0, %5, 0\n\ts_load_dwordx8 %1, %6, 0\n\ts_load_dwordx2 %2, %6, 32\n\t"
               "s_load_dwordx8 %3, %7, 0\n\ts_load_dwordx2 %4, %7, 32"
               : "=&s"(epoch), "=&s"(g8), "=&s"(g2_), "=&s"(b8), "=&s"(b2_)
               : "s"((size_t)(sy + SY_EPOCH)), "s"((size_t)(g1 + ns * 80 + w * 20)), "s"((size_t)(be1 + ns * 80 + w * 20)),
                 "s"((size_t)w1), "s"((size_t)wemb), "s"((size_t)temb));
  unsigned ka;
  asm volatile("s_load_dword %0, %1, 64" : "=s"(ka) : "s"((size_t)__builtin_amdgcn_kernarg_segment_ptr()));
  __builtin_amdgcn_sched_barrier(0);
  const int ej = bid - 200 + (w - 1) * 56;
  const bool jw = w == 1 && bid < 200, je = bid >= 200 && (w == 1 || (w == 2 && ej < 80));
  const bool jw2 = w == 2 && bid < 200;
  const int ep = ej / 5, ecg = ej % 5;
  u32x4 jb[9];
  unsigned short tv[9];
  Job J;
  {
    const int ot = bid / 10, jq = bid % 10, oc = jq * 4 + kg;
    const int kq = oc / 10, rr = oc % 10, slab = rr >> 1;
    const unsigned dst = (unsigned)(w == 1 ? OFF_WT1 : OFF_WT2) + ((ot / 5) * 4 + kq) * WQ_B + (ot % 5) * 1024 + l15 * 16 + (rr & 1) * 256;
    J.src = ((ot * 16 + l15) * 2880 + oc * 72) * 2;
    const __amdgpu_buffer_rsrc_t rj = mk_rsrc(je ? wemb : w1, jw ? 320 * 2880 * 2 : (je ? NEMB * NCH * 2 : 0));
    const __amdgpu_buffer_rsrc_t rt_ = mk_rsrc(temb, je ? 2 * NEMB * 2 : 0);
    const unsigned jo = je ? ((ep * 72 + (lane >> 3)) * NCH + ecg * 64 + (lane & 7) * 8) * 2 : J.src, js = je ? 8 * NCH * 2 : 16;
#pragma unroll
    for (int k = 0; k < 9; ++k) tv[k] = __builtin_amdgcn_raw_buffer_load_b16(rt_, ((lane & 1) * NEMB + ep * 72 + (lane >> 3)) * 2, k * 16, 0);
    __builtin_amdgcn_sched_barrier(0);
#pragma unroll
    for (int k = 0; k < 9; ++k) jb[k] = __builtin_amdgcn_raw_buffer_load_b128(rj, jo, k * js, 0);
    __builtin_amdgcn_sched_barrier(0);
    J.dstS = slab == 0 ? dst : 0x80000000u;
    J.dstP = slab == 0 ? 0u : dst + (5 + 9 * ((slab - 1) >> 1)) * 5120 + ((slab - 1) & 1) * 512;
    const unsigned fq = jq - ((5 * kg) >> 1);
    J.flag = fq < 3u ? (unsigned)(OFF_SYNC + SY_FJ) + (w == 1 ? 0 : 65536) + (((ot / 5) * 4 + kg) * 16 + l15) * 256 + ((ot % 5) * 3 + fq) * 8
                     : 0x80000000u;
  }
  const __amdgpu_buffer_rsrc_t rsS = mk_rsrc(ws, (unsigned)(OFF_SYNC + SY_END));
  const unsigned dst1 = (unsigned)OFF_ACT1 + (b * 20 + ns * 5) * PLANE_B + (rp * 128 + (w & 2) * 32) * 32 + lane * 16;
  const unsigned dst2 = (unsigned)OFF_ACT2 + (b * 20 + ns * 5) * PLANE_B + ((y0 + 2 * w + (kg & 1)) * 64 + x0 + l15) * 32 + (kg >> 1) * 16;

  Geo G;
  G.w = w; G.lane = lane;
  G.ldsb = (unsigned)(size_t)(LDS_AS char*)lds;
  G.wreg = G.ldsb + w * WREG_B;
  G.wvo = lane * 16;
  G.avo = (unsigned)((((b * NCH + ns * 80 + l15) * 64 + y0 + 2 * w + (kg & 1)) * 64 + x0 + (kg >> 1) * 8) * 2);
#pragma unroll
  for (int j = 0; j < 6; ++j) {
    const int u = 64 * j + lane, p = u >> 1;
    const int row = (p * 3641) >> 16, col = p - row * 18;
    const int gy = y0 - 1 + row, gx = x0 - 1 + col;
    const bool ok = p < 180 && (unsigned)gy < 64u && (unsigned)gx < 64u;
    G.dvo[j] = ok ? (unsigned)((gy * 64 + gx) * 32 + (u & 1) * 16) : 0x80000000u;
  }
#pragma unroll
  for (int k = 0; k < 4; ++k) {
    const unsigned base = G.wreg + l15 * 32 + (kg & 1) * 16 + ((w + 1 + k) & 3) * 1152;
    G.vb[0][k] = base + (kg >> 1) * 576;
    G.vb[1][k] = base + (kg >> 1) * 32;
    G.vb[2][k] = base + (kg >> 1) * SLAB_B;
  }
  const unsigned ldsb = G.ldsb, wreg = G.wreg;

  {
    float s = 0.f;
#pragma unroll
    for (int i = 0; i < 20; ++i) s += bf_lo(xv[i]) + bf_hi(xv[i]);
    const float mean = wave_sum(s) * (1.0f / 2560.0f);
    float m2 = 0.f;
#pragma unroll
    for (int i = 0; i < 20; ++i) {
      float a = bf_lo(xv[i]) - mean, c = bf_hi(xv[i]) - mean;
      m2 += a * a + c * c;
    }
    m2 = wave_sum(m2);
    if (lane == 0) *(LDS_AS f32x4*)(size_t)(wreg + RED_OFF) = (f32x4){mean, m2, 0.f, -1.f};
  }
  asm volatile("s_waitcnt lgkmcnt(0)" : "+s"(epoch), "+s"(g8), "+s"(g2_), "+s"(b8), "+s"(b2_), "+s"(ka));
  unsigned gs[10], bs[10];
#pragma unroll
  for (int i = 0; i < 10; ++i) { gs[i] = i < 8 ? g8[i & 7] : g2_[i & 1]; bs[i] = i < 8 ? b8[i & 7] : b2_[i & 1]; }
  const u64 tag = epoch + 0x9E3779B97F4A7C15ull;
  wg_barrier();
  if (w == 0) {
    publish_stats(ldsb, sy + SY_ST1 + (b * 4 + ns) * 16384 + rp * 16, tag, lane);
    gather_stats(ldsb, sy + SY_ST1 + (b * 4 + ns) * 16384 + rp * 512, tag, lane);
  } else if (jw) {
    sfor<9>([&](auto T) { st16_dev(rt_tap<T>(jb), rsS, rt_off<T>(J), 0); });
    st16_dev((u32x4){0u, 0u, 0u, 0u}, rsS, rt_off<9>(J), 0);
  } else if (je) {
    float acc[2][8];
#pragma unroll
    for (int i = 0; i < 8; ++i) acc[0][i] = acc[1][i] = 0.f;
#pragma unroll
    for (int k = 0; k < 9; ++k) {
      const float so = silu_f(bf_lo(tv[k]));
      const float sx = __builtin_bit_cast(float, __builtin_amdgcn_update_dpp(0, __builtin_bit_cast(int, so), 0xB1, 0xf, 0xf, true));
      const float s0 = (lane & 1) ? sx : so, s1 = (lane & 1) ? so : sx;
#pragma unroll
      for (int q = 0; q < 4; ++q) {
        const float a = bf_lo(jb[k][q]), c = bf_hi(jb[k][q]);
        acc[0][2 * q] += s0 * a; acc[0][2 * q + 1] += s0 * c;
        acc[1][2 * q] += s1 * a; acc[1][2 * q + 1] += s1 * c;
      }
    }
    float c8[8];
#pragma unroll
    for (int i = 0; i < 8; ++i) {
      u32x2 t = __builtin_amdgcn_permlane16_swap(__float_as_uint(acc[0][i]), __float_as_uint(acc[1][i]), false, false);
      c8[i] = __uint_as_float(t.x) + __uint_as_float(t.y);
    }
    u32x4 o;
#pragma unroll
    for (int i = 0; i < 4; ++i) {
      u32x2 t = __builtin_amdgcn_permlane32_swap(__float_as_uint(c8[i]), __float_as_uint(c8[i + 4]), false, false);
      float d = __uint_as_float(t.x) + __uint_as_float(t.y);
      d = dpp_add<0x128>(d);
      o[i] = __float_as_uint(d);
    }
    if (l15 < 8) st16_dev(o, rsS, (unsigned)OFF_EMB + ((ep * 2 + (kg & 1)) * NCH + ecg * 64 + l15 * 8 + (kg >> 1) * 4) * 4, 0);
    J.flag = lane < 32 && (unsigned)(ecg - kg) < 4u ? (unsigned)(OFF_SYNC + SY_FE) + ((ecg - kg) * 16 + l15) * 256 + (kg * 16 + ep) * 8
                                                    : 0x80000000u;
  }
  asm volatile("" ::"v"(cwarm));
  wg_barrier();

  const unsigned fjo = (unsigned)SY_FJ + ((ns * 4 + w) * 16 + b * 8 + ty) * 256 + l15 * 8;
  u32x4 Wp[NPW][5], Ax[5];
  f32x4 v[2][5];
  if (jw2) {
#pragma unroll
      for (int k = 0; k < 9; ++k) jb[k] = __builtin_amdgcn_raw_buffer_load_b128(mk_rsrc(w2, 320 * 2880 * 2), J.src + k * 16, 0, 0);
    }
  u64 fj1 = 0;
  u32x2 gv[5], bv[5];
  const __amdgpu_buffer_rsrc_t rsW = mk_rsrc(ws + OFF_WT1 + (size_t)(ns * 4 + w) * WQ_B, WQ_B);
  {
    const f32x2 st = *(const LDS_AS f32x2*)(size_t)(ldsb + RED_OFF + 8);
#define PREF(k_) do { constexpr int k = k_; Wp[k / 5][k % 5] = __builtin_amdgcn_raw_buffer_load_b128(rsW, G.wvo + k * 1024, 0, 0); } while (0)
    const unsigned stg = ldsb + WREG_B;
    sfor<5>([&](auto Q) {
      constexpr int q = Q;
      unsigned o[2][2];
#pragma unroll
      for (int h = 0; h < 2; ++h) {
        const unsigned gg = gs[2 * q + h], bb = bs[2 * q + h];
        const float a0 = st.y * bf_lo(gg), a1 = st.y * bf_hi(gg);
        const float b0 = bf_lo(bb) - st.x * a0, b1 = bf_hi(bb) - st.x * a1;
        const unsigned u0 = xv[4 * q + 2 * h], u1 = xv[4 * q + 2 * h + 1];
        o[0][h] = pk_bf16(silu_f(bf_lo(u0) * a0 + b0), silu_f(bf_lo(u1) * a1 + b1));
        o[1][h] = pk_bf16(silu_f(bf_hi(u0) * a0 + b0), silu_f(bf_hi(u1) * a1 + b1));
      }
      const int c = w * 20 + q * 4;
      const unsigned ad = stg + (c >> 4) * 4096 + lane * 64 + (c & 4) * 2;
#pragma unroll
      for (int j = 0; j < 2; ++j)
        *(LDS_AS u32x2*)(size_t)(ad + (((j * 2 + ((c >> 3) & 1)) ^ ((lane >> 2) & 3)) << 4)) = (u32x2){o[j][0], o[j][1]};
      __builtin_amdgcn_sched_barrier(0);
      if constexpr (q == FLQ) {
        if (jw || je) { release_dev(); bstore8_dev(tag, J.flag, rsS); }
        __builtin_amdgcn_sched_barrier(0);
      }
    });
    wg_barrier();
    if (w == 0 || w == 3) {
#pragma unroll
      for (int m = 0; m < 10; ++m) {
        const u32x4 q = *(const LDS_AS u32x4*)(size_t)(stg + (m >> 1) * 4096 + ((w & 2) + (m & 1)) * 1024 + ((lane & ~3) << 4) + (((lane & 3) ^ kg) << 4));
        st16_act(q, rsS, dst1 + (m & 1) * 1024, (m >> 1) * PLANE_B);
      }
    }
    if (FJE) fj1 = ld_dev(sy + fjo);
    if (w == 0 || w == 3) release_dev();
  }
  wg_barrier();
  if (tid < 8) {
    const int r3 = rp & 3, up = tid >> 2;
    const int cty = (rp >> 2) + (up ? (r3 == 0 ? -1 : 1) : 0), slot = up ? (r3 == 0 ? 5 : 0) : r3 + 1;
    if (!up || ((r3 == 0 || r3 == 3) && (unsigned)cty < 8u))
      st_dev(sy + SY_MB1 + (((b * 8 + cty) * 4 + (tid & 3)) * 4 + ns) * 256 + slot * 8, tag);
  }
  wait_words(sy + fjo, l15 < 15, tag, fj1);
  sfor<NPW1 * 5>([&](auto K) { PREF(K); });
  const __amdgpu_buffer_rsrc_t rsW2 = mk_rsrc(ws + OFF_WT2 + (size_t)(ns * 4 + w) * WQ_B, WQ_B);
  {
    const int prp = 4 * ty - 1 + lane;
    const bool on = lane < 6 && (unsigned)prp < 32u;
    const char* fp = sy + SY_MB1 + (((b * 8 + ty) * 4 + tx) * 4 + w) * 256 + (lane & 15) * 8;
    wait_words(fp, on, tag, ld_dev(fp));
    acquire_dev();
#pragma unroll
    for (int k = 0; k < NPW1; ++k) wait_vm5<-1>(Wp[k]);
    asm volatile("" : "+v"(jb[0]), "+v"(jb[1]), "+v"(jb[2]), "+v"(jb[3]), "+v"(jb[4]), "+v"(jb[5]), "+v"(jb[6]), "+v"(jb[7]), "+v"(jb[8]));
    conv_main<1>(G, mk_rsrc(ws + OFF_ACT1, ACT_B), (unsigned)((b * 20 + w * 5) * PLANE_B), rsW,
                 rsX, Wp, Ax, v, J, jb, mk_rsrc(ws, jw2 ? (unsigned)(OFF_SYNC + SY_END) : 0u), rsS,
                 (unsigned)OFF_SYNC + (lane < 32 ? fjo + 65536 : (unsigned)SY_FE + (ns * 16 + b * 8 + ty) * 256 + (lane & 31) * 8),
                 (unsigned)OFF_EMB + ((l15 * 2 + b) * NCH + ns * 80 + kg * 4) * 4, tag, rsW2);
  }

  {
    float s = 0.f;
#pragma unroll
    for (int n = 0; n < 5; ++n)
#pragma unroll
      for (int e = 0; e < 2; ++e) s += (v[e][n][0] + v[e][n][1]) + (v[e][n][2] + v[e][n][3]);
    const float mean = wave_sum(s) * (1.0f / 2560.0f);
    float m2 = 0.f;
#pragma unroll
    for (int n = 0; n < 5; ++n)
#pragma unroll
      for (int e = 0; e < 2; ++e)
#pragma unroll
        for (int j = 0; j < 4; ++j) { float d = v[e][n][j] - mean; m2 += d * d; }
    m2 = wave_sum(m2);
    if (lane == 0) *(LDS_AS f32x2*)(size_t)(wreg + RED_OFF) = (f32x2){mean, m2};
  }
  wg_barrier();
  if (w == 0) publish_stats(ldsb, sy + SY_ST2 + (b * 4 + ns) * 16384 + (ty * 4 + tx) * 16, tag, lane);
#pragma unroll
  for (int n = 0; n < 5; ++n) {
    gv[n] = *(const u32x2*)(g2 + ns * 80 + n * 16 + kg * 4);
    bv[n] = *(const u32x2*)(be2 + ns * 80 + n * 16 + kg * 4);
  }
  if (w == 0) gather_stats(ldsb, sy + SY_ST2 + (b * 4 + ns) * 16384 + (ty * 4 + tx) * 512, tag, lane);
  if (bid == 0 && w == 1) {
    const unsigned t32 = (lane & 1) ? (unsigned)(tag >> 32) : (unsigned)tag;
#pragma unroll 1
    for (int it = 0; it < SPIN_MAX; ++it) {
      bool miss = false;
#pragma unroll
      for (int k = 0; k < 8; ++k) miss |= (unsigned)(ld_dev(sy + SY_ST1 + k * 16384 + lane * 8) >> 32) != t32;
      if (__builtin_amdgcn_ballot_w64(miss) == 0) break;
      __builtin_amdgcn_s_sleep(8);
    }
    if (lane == 0) st_dev(sy + SY_EPOCH, tag - 0x9E3779B97F4A7C15ull + 0x100000001ull);
  }
  wg_barrier();
  {
    const f32x2 st = *(const LDS_AS f32x2*)(size_t)(ldsb + RED_OFF + 8);
#pragma unroll
    for (int n = 0; n < 5; ++n) {
      unsigned o[2][2];
#pragma unroll
      for (int h = 0; h < 2; ++h) {
        const float a0 = st.y * bf_lo(gv[n][h]), a1 = st.y * bf_hi(gv[n][h]);
        const float b0 = bf_lo(bv[n][h]) - st.x * a0, b1 = bf_hi(bv[n][h]) - st.x * a1;
#pragma unroll
        for (int e = 0; e < 2; ++e) o[e][h] = pk_bf16(silu_f(v[e][n][2 * h] * a0 + b0), silu_f(v[e][n][2 * h + 1] * a1 + b1));
      }
      u32x2 s0 = __builtin_amdgcn_permlane16_swap(o[0][0], o[1][0], false, false);
      u32x2 s1 = __builtin_amdgcn_permlane16_swap(o[0][1], o[1][1], false, false);
      st16_act((u32x4){s0.x, s1.x, s0.y, s1.y}, rsS, dst2, n * PLANE_B);
    }
  }
  release_dev();
  wg_barrier();
  {
    const int nty = ty - 1 + lane / 3, ntx = tx - 1 + lane % 3;
    const bool on = lane < 9 && (unsigned)nty < 8u && (unsigned)ntx < 4u;
    if (w == 0 && on) st_dev(sy + SY_MB2 + (((b * 8 + nty) * 4 + ntx) * 4 + ns) * 256 + (8 - lane) * 8, tag);
    const char* fp = sy + SY_MB2 + (((b * 8 + ty) * 4 + tx) * 4 + w) * 256 + (lane & 15) * 8;
    wait_words(fp, on, tag, ld_dev(fp));
    acquire_dev();
    conv_main<2>(G, mk_rsrc(ws + OFF_ACT2, ACT_B), (unsigned)((b * 20 + w * 5) * PLANE_B), rsW2, rsX, Wp, Ax, v,
                 J, jb, rsS, rsS, 0u, 0u, tag, rsS);
  }
  {
    char* dst = outp + G.avo;
#pragma unroll
    for (int n = 0; n < 5; ++n) {
      float lo[4], hi[4];
#pragma unroll
      for (int j = 0; j < 4; ++j) {
        u32x2 t = __builtin_amdgcn_permlane16_swap(__float_as_uint(v[0][n][j]), __float_as_uint(v[1][n][j]), false, false);
        lo[j] = __uint_as_float(t.x); hi[j] = __uint_as_float(t.y);
      }
      u32x4 o;
      o.x = pk_bf16(lo[0] + bf_lo(Ax[n].x), lo[1] + bf_hi(Ax[n].x));
      o.y = pk_bf16(lo[2] + bf_lo(Ax[n].y), lo[3] + bf_hi(Ax[n].y));
      o.z = pk_bf16(hi[0] + bf_lo(Ax[n].z), hi[1] + bf_hi(Ax[n].z));
      o.w = pk_bf16(hi[2] + bf_lo(Ax[n].w), hi[3] + bf_hi(Ax[n].w));
      *(u32x4*)(dst + (size_t)n * (16 * NPX * 2)) = o;
    }
  }
}

extern "C" void kernel_launch(void* const* d_in, const int* in_sizes, int n_in, void* d_out, int out_size, void* d_ws,
                              size_t ws_size, hipStream_t stream) {
  resblock_k<<<256, 256, 0, stream>>>((const uint16_t*)d_in[0], (char*)d_ws, (const uint16_t*)d_in[2],
                                      (const uint16_t*)d_in[3], (const uint16_t*)d_in[4], (const uint16_t*)d_in[5],
                                      (const uint16_t*)d_in[1], (const uint16_t*)d_in[8], (const uint16_t*)d_in[6],
                                      (const uint16_t*)d_in[7], (char*)d_out);
}
```
